```python
import torch

def _softmax(x, axis=-1):
    return torch.softmax(x, dim=axis)
def _gelu(x):
    return torch.nn.functional.gelu(x, approximate="tanh")
def _silu(x):
    return torch.nn.functional.silu(x)
def _sigmoid(x):
    return torch.sigmoid(x)
def _layernorm(x, axis=-1, eps=1e-5):
    m = x.mean(dim=axis, keepdim=True)
    v = x.var(dim=axis, keepdim=True, unbiased=False)
    return (x - m) / torch.sqrt(v + eps)
def _scatter_add(tgt, idx, src, axis=0):
    return tgt.clone().index_add_(axis, idx, src)
def _segment_sum(src, seg, n):
    out = torch.zeros((n,) + src.shape[1:], dtype=src.dtype)
    return out.index_add_(0, seg, src)
def _pad(x, pw):
    flat = tuple(v for pair in reversed(pw) for v in pair)
    return torch.nn.functional.pad(x, flat)

kernel_name = 'concat_heads__grouped_kv_mqa__softmax_full_mha__prompt'

def setup_inputs():

    _w = (lambda a: (lambda t: t.to(torch.bfloat16) if t.is_floating_point() else t)(torch.as_tensor(a)))
    def _A_setup():
        B, S, D, H = 1, 2048, 1024, 32
        Dh = D // H
        return {
            "x": _w((lambda *s: torch.randn(*s, dtype=torch.bfloat16) * 0.05)(B, S, D)),
            "wq": _w((lambda *s: torch.randn(*s, dtype=torch.bfloat16) * 0.8)(D, H * Dh)),
            "wk": _w((lambda *s: torch.randn(*s, dtype=torch.bfloat16) * 0.8)(D, Dh)),
            "wv": _w((lambda *s: torch.randn(*s, dtype=torch.bfloat16) * 0.1)(D, Dh)),
            "wo": _w((lambda *s: torch.randn(*s, dtype=torch.bfloat16) * 0.1)(H * Dh, D)),
        }
    _A_in = _A_setup()

    def _B_setup():
        B, S, D = 1, 2048, 1024
        return {
            "x": _w((lambda *s: torch.randn(*s, dtype=torch.bfloat16) * 0.2)(B, S, D)),
            "wqkv": _w((lambda *s: torch.randn(*s, dtype=torch.bfloat16) * 0.2)(D, 3 * D)),
            "wo": _w((lambda *s: torch.randn(*s, dtype=torch.bfloat16) * 0.05)(D, D)),
        }
    _B_in = _B_setup()

    return {
        "A_x": _A_in["x"],
        "A_wq": _A_in["wq"],
        "A_wk": _A_in["wk"],
        "A_wv": _A_in["wv"],
        "A_wo": _A_in["wo"],
        "B_x": _B_in["x"],
        "B_wqkv": _B_in["wqkv"],
        "B_wo": _B_in["wo"],
    }

def reference(A_x, A_wq, A_wk, A_wv, A_wo, B_x, B_wqkv, B_wo):
    def _A_ref(x, wq, wk, wv, wo):
        B, S, D = x.shape
        H = 32
        Dh = D // H
        q = (lambda x, *d: x.permute(*d))((x @ wq).reshape(B, S, H, Dh), 0, 2, 1, 3)
        k = x @ wk
        v = x @ wv
        scores = torch.einsum("bhqd,bkd->bhqk", q, k) * (Dh ** -0.5)
        p = _softmax(scores, axis=-1)
        o = torch.einsum("bhqk,bkd->bhqd", p, v)
        return (lambda x, *d: x.permute(*d))(o, 0, 2, 1, 3).reshape(B, S, H * Dh) @ wo

    def _B_ref(x, wqkv, wo):
        B, S, D = x.shape
        H = 32; Dh = D // H
        qkv = (lambda x, *d: x.permute(*d))((x @ wqkv).reshape(B, S, 3, H, Dh), 2, 0, 3, 1, 4)
        q, k, v = qkv[0], qkv[1], qkv[2]
        scale = Dh ** -0.5
        scores = torch.einsum("bhqd,bhkd->bhqk", q, k) * scale
        p = _softmax(scores, axis=-1)
        o = torch.einsum("bhqk,bhkd->bhqd", p, v)
        return (lambda x, *d: x.permute(*d))(o, 0, 2, 1, 3).reshape(B, S, D) @ wo

    a = _A_ref(A_x, A_wq, A_wk, A_wv, A_wo)
    b = _B_ref(B_x, B_wqkv, B_wo)
    return torch.concatenate([a, b], axis=-1)
```

```cpp
#include <hip/hip_runtime.h>
#include <hip/hip_bf16.h>
#include <stdint.h>

typedef short    s16x4  __attribute__((ext_vector_type(4)));
typedef short    bf16x8 __attribute__((ext_vector_type(8)));
typedef __bf16   bf16x2 __attribute__((ext_vector_type(2)));
typedef float    f32x2  __attribute__((ext_vector_type(2)));
typedef float    f32x4  __attribute__((ext_vector_type(4)));
typedef float    f32x16 __attribute__((ext_vector_type(16)));
typedef unsigned u32x4  __attribute__((ext_vector_type(4)));
typedef unsigned u32x2  __attribute__((ext_vector_type(2)));
typedef __attribute__((address_space(3))) const char* lds_cptr;
typedef short v4i16_t __attribute__((ext_vector_type(4)));
#define DI __device__ __forceinline__
#define MFMA32(a, b, c) __builtin_amdgcn_mfma_f32_32x32x16_bf16((a), (b), (c), 0, 0, 0)

constexpr int S = 2048, D = 1024, H = 32;
constexpr float QSCALE = 0.17677669529663687f * 1.4426950408889634f;

constexpr size_t WS_QA  = 0;
constexpr size_t WS_QB  = WS_QA + (4u << 20);
constexpr size_t WS_KVA = WS_QB + (4u << 20);
constexpr size_t WS_KVB = WS_KVA + (256u << 10);
constexpr size_t WS_O   = WS_KVB + (8u << 20);

DI unsigned cvtpk(float lo, float hi) { f32x2 v = {lo, hi}; bf16x2 b = __builtin_convertvector(v, bf16x2); return __builtin_bit_cast(unsigned, b); }
DI u32x4 pack8(const f32x16& x, int s, float sc) {
  u32x4 p;
  p[0] = cvtpk(x[8 * s + 0] * sc, x[8 * s + 1] * sc); p[1] = cvtpk(x[8 * s + 2] * sc, x[8 * s + 3] * sc);
  p[2] = cvtpk(x[8 * s + 4] * sc, x[8 * s + 5] * sc); p[3] = cvtpk(x[8 * s + 6] * sc, x[8 * s + 7] * sc);
  return p;
}
DI void glds16(const void* sbase, unsigned voff, unsigned lds_base) {
  unsigned sv;
  asm volatile("s_mov_b32 %0, m0\n\ts_mov_b32 m0, %3\n\ts_nop 0\n\tglobal_load_lds_dwordx4 %1, %2\n\ts_mov_b32 m0, %0"
               : "=&s"(sv) : "v"(voff), "s"(sbase), "s"(lds_base) : "memory");
}
DI bf16x8 lds_b128(lds_cptr p) { return *(const __attribute__((address_space(3))) bf16x8*)p; }
DI s16x4 lds_tr(lds_cptr p) { return __builtin_bit_cast(s16x4, __builtin_amdgcn_ds_read_tr16_b64_v4i16((__attribute__((address_space(3))) v4i16_t*)p)); }
DI bf16x8 cat8(s16x4 lo, s16x4 hi) { return __builtin_shufflevector(lo, hi, 0, 1, 2, 3, 4, 5, 6, 7); }
DI unsigned rfl(unsigned x) { return __builtin_amdgcn_readfirstlane(x); }
#define WAIT_BAR(N) asm volatile("s_waitcnt vmcnt(" #N ") lgkmcnt(0)\n\ts_barrier" ::: "memory")

constexpr int P_XB = 32768, P_WB = 16384, P_SLOT = P_XB + P_WB, P_KV = 3 * P_SLOT, P_KVB = 4096, P_LDS = P_KV + 3 * P_KVB;

template <int MODE, bool EXTRA>
DI void proj_body(const char* __restrict__ X, const char* __restrict__ W, int ldw  , const char* __restrict__ Wx  , bool extra_is_v,
                  char* dst  , char* dst_extra, int mblk, int ntile, char* lds) {
  const int tid = threadIdx.x, lane = tid & 63, wid = rfl(tid >> 6), wm = wid >> 1, wn = wid & 1, h = lane >> 5;
  const unsigned lds0 = (unsigned)(uintptr_t)lds;
  const unsigned xrow = 32 * wid + (lane >> 3);
  const unsigned xo_e = xrow * 2048u + (((lane & 7) ^ ((lane >> 4) & 7)) << 4);
  const unsigned xo_o = (xrow + 8) * 2048u + (((lane & 7) ^ (4 + (lane >> 4))) << 4);
  const char* xbase = X + (size_t)mblk * 256 * 2048;
  unsigned wo[2];
#pragma unroll
  for (int i = 0; i < 2; ++i) { const unsigned row = 8 * wid + 4 * i + (lane >> 4); const unsigned g = ((row & 3) << 2) | ((row >> 2) & 3);
    wo[i] = row * (unsigned)ldw + (((lane & 15) ^ g) << 4); }
  const unsigned kvo = (16 * (wid & 3) + (lane >> 2)) * 64u + (lane & 3) * 16u;
  auto stage_issue = [&](int st) {
    const unsigned slot = lds0 + (st % 3) * P_SLOT;
    const char* xb = xbase + st * 128;
    glds16(xb, xo_e, slot + wid * 4096);           glds16(xb, xo_o, slot + wid * 4096 + 1024);
    glds16(xb + 32768, xo_e, slot + wid * 4096 + 2048); glds16(xb + 32768, xo_o, slot + wid * 4096 + 3072);
    const char* wb = W + (size_t)st * 64 * ldw;
    glds16(wb, wo[0], slot + P_XB + wid * 2048);   glds16(wb, wo[1], slot + P_XB + wid * 2048 + 1024);
    if (EXTRA) glds16(Wx + st * 4096, kvo, lds0 + P_KV + (st % 3) * P_KVB + (wid & 3) * 1024);
  };
  const int r32 = lane & 31;
  unsigned xa[4];
#pragma unroll
  for (int s = 0; s < 4; ++s) xa[s] = (64 * wm + r32) * 128 + (((2 * s + h) ^ ((r32 >> 1) & 7)) << 4);
  const int blk = (lane >> 4) & 1, q = (lane & 15) >> 2, p = lane & 3;
  unsigned wa[2][2];
#pragma unroll
  for (int c = 0; c < 2; ++c)
#pragma unroll
    for (int t = 0; t < 2; ++t) { const int row = 8 * h + 4 * t + q; const int ch = (4 * (2 * wn + c) + 2 * blk + (p >> 1)) ^ ((q << 2) | ((2 * h + t) & 3));
      wa[c][t] = P_XB + row * 256 + ch * 16 + 8 * (p & 1); }
  const unsigned kva = (8 * h + q) * 64 + blk * 32 + p * 8;
  const bool do_extra = EXTRA && (wm == (ntile >> 1));

  f32x16 acc[2][2] = {}; f32x16 accx = {};
  stage_issue(0); stage_issue(1);
  for (int st = 0; st < 16; ++st) {
    if (st < 15) { if (EXTRA) WAIT_BAR(7); else WAIT_BAR(6); } else WAIT_BAR(0);
    if (st + 2 < 16) stage_issue(st + 2);
    const lds_cptr sl = (lds_cptr)lds + (st % 3) * P_SLOT;
    const lds_cptr kv = (lds_cptr)lds + P_KV + (st % 3) * P_KVB;
#pragma unroll
    for (int s = 0; s < 4; ++s) {
      const bf16x8 x0 = lds_b128(sl + xa[s]), x1 = lds_b128(sl + xa[s] + 4096);
      const bf16x8 w0 = cat8(lds_tr(sl + wa[0][0] + s * 4096), lds_tr(sl + wa[0][1] + s * 4096));
      const bf16x8 w1 = cat8(lds_tr(sl + wa[1][0] + s * 4096), lds_tr(sl + wa[1][1] + s * 4096));
      if (MODE != 2) {
        acc[0][0] = MFMA32(w0, x0, acc[0][0]); acc[0][1] = MFMA32(w0, x1, acc[0][1]);
        acc[1][0] = MFMA32(w1, x0, acc[1][0]); acc[1][1] = MFMA32(w1, x1, acc[1][1]);
      } else {
        acc[0][0] = MFMA32(x0, w0, acc[0][0]); acc[0][1] = MFMA32(x1, w0, acc[0][1]);
        acc[1][0] = MFMA32(x0, w1, acc[1][0]); acc[1][1] = MFMA32(x1, w1, acc[1][1]);
      }
      if (EXTRA) { if (do_extra) {
        const bf16x8 wx = cat8(lds_tr(kv + kva + s * 1024), lds_tr(kv + kva + s * 1024 + 256));
        const bf16x8 xx = wn ? x1 : x0;
        if (extra_is_v) accx = MFMA32(xx, wx, accx); else accx = MFMA32(wx, xx, accx);
      } }
    }
  }
  const float sc = (MODE == 0) ? QSCALE : 1.f;
#pragma unroll
  for (int c = 0; c < 2; ++c)
#pragma unroll
    for (int tb = 0; tb < 2; ++tb) {
      const int head = 2 * wn + c, t = mblk * 8 + 2 * wm + tb;
      char* o = dst + ((size_t)head * 64 + t) * (MODE == 0 ? 2048 : 4096) + lane * 16;
      *(u32x4*)o = pack8(acc[c][tb], 0, sc); *(u32x4*)(o + 1024) = pack8(acc[c][tb], 1, sc);
    }
  if (EXTRA) { if (do_extra) {
    const int t = mblk * 8 + 2 * wm + wn;
    char* o = dst_extra + (size_t)t * 4096 + (extra_is_v ? 2048 : 0) + lane * 16;
    *(u32x4*)o = pack8(accx, 0, 1.f); *(u32x4*)(o + 1024) = pack8(accx, 1, 1.f);
  } }
}

__global__ __launch_bounds__(512, 2) void k_proj(const char* __restrict__ xa, const char* __restrict__ wq, const char* __restrict__ wk, const char* __restrict__ wv,
                                                 const char* __restrict__ xb, const char* __restrict__ wqkv, char* __restrict__ ws) {
  extern __shared__ __attribute__((aligned(16))) char lds[];
  const int xcd = blockIdx.x & 7, idx = blockIdx.x >> 3, xm = xcd & 1, xn = xcd >> 1;
  if (idx < 24) {
    const int mblk = 4 * xm + (idx & 3), nt = 6 * xn + (idx >> 2);
    const char* W = wqkv + nt * 256;
    if (nt < 8)       proj_body<0, false>(xb, W, 6144, nullptr, false, ws + WS_QB + (size_t)nt * 4 * 64 * 2048, nullptr, mblk, nt, lds);
    else if (nt < 16) proj_body<1, false>(xb, W, 6144, nullptr, false, ws + WS_KVB + (size_t)(nt - 8) * 4 * 64 * 4096, nullptr, mblk, nt, lds);
    else              proj_body<2, false>(xb, W, 6144, nullptr, false, ws + WS_KVB + (size_t)(nt - 16) * 4 * 64 * 4096 + 2048, nullptr, mblk, nt, lds);
  } else {
    const int j = idx - 24, mblk = 4 * xm + (j & 3), nt = 2 * xn + (j >> 2);
    proj_body<0, true>(xa, wq + nt * 256, 2048, (nt & 1) ? wv : wk, (nt & 1) != 0, ws + WS_QA + (size_t)nt * 4 * 64 * 2048, ws + WS_KVA, mblk, nt, lds);
  }
}

DI void attn_slow(const char* __restrict__ qf  , const char* __restrict__ kv, char* __restrict__ o0, char* __restrict__ o1, int lane) {
#pragma unroll 1
  for (int b = 0; b < 2; ++b) {
    const bf16x8 q0 = *(const bf16x8*)(qf + b * 2048 + lane * 16), q1 = *(const bf16x8*)(qf + b * 2048 + 1024 + lane * 16);
    float m = -INFINITY, l = 0.f; f32x16 o = {};
#pragma unroll 1
    for (int t = 0; t < 64; ++t) {
      const char* kp = kv + t * 4096 + lane * 16;
      const bf16x8 k0 = *(const bf16x8*)kp, k1 = *(const bf16x8*)(kp + 1024), v0 = *(const bf16x8*)(kp + 2048), v1 = *(const bf16x8*)(kp + 3072);
      f32x16 s = {}; s = MFMA32(k0, q0, s); s = MFMA32(k1, q1, s);
      float tm = s[0];
#pragma unroll
      for (int i = 1; i < 16; ++i) tm = fmaxf(tm, s[i]);
      tm = fmaxf(tm, __shfl_xor(tm, 32));
      const float mn = fmaxf(m, tm);
      const float alpha = __builtin_amdgcn_exp2f(m - mn);
      m = mn;
      float ps = 0.f;
#pragma unroll
      for (int i = 0; i < 16; ++i) { s[i] = __builtin_amdgcn_exp2f(s[i] - mn); ps += s[i]; }
      l = l * alpha + ps;
#pragma unroll
      for (int i = 0; i < 16; ++i) o[i] *= alpha;
      const u32x4 p0 = pack8(s, 0, 1.f), p1 = pack8(s, 1, 1.f);
      o = MFMA32(v0, __builtin_bit_cast(bf16x8, p0), o); o = MFMA32(v1, __builtin_bit_cast(bf16x8, p1), o);
    }
    l += __shfl_xor(l, 32);
    const float rl = 1.f / l;
    char* op = (b ? o1 : o0) + lane * 16;
    *(u32x4*)op = pack8(o, 0, rl); *(u32x4*)(op + 1024) = pack8(o, 1, rl);
  }
}

__global__ __launch_bounds__(512, 2) void k_attn(char* __restrict__ ws) {
  const int xcd = blockIdx.x & 7, idx = blockIdx.x >> 3, br = idx >> 4, i = idx & 15;
  const int head = xcd * 4 + (i >> 2), quarter = i & 3;
  const int tid = threadIdx.x, lane = tid & 63, wid = rfl(tid >> 6);
  const int t0 = quarter * 16 + 2 * wid;
  const char* qf = ws + (br ? WS_QB : WS_QA) + ((size_t)head * 64 + t0) * 2048;
  const char* kv = br ? ws + WS_KVB + (size_t)head * 64 * 4096 : ws + WS_KVA;
  char* o0 = ws + WS_O + (((size_t)br * 64 + t0) * 32 + head) * 2048;
  attn_slow(qf, kv, o0, o0 + 32 * 2048, lane);
}

constexpr int O_XB = 16384, O_WB = 16384, O_SLOT = O_XB + O_WB, O_LDS = 4 * O_SLOT;
__global__ __launch_bounds__(256, 1) void k_out(const char* __restrict__ ws, const char* __restrict__ woa, const char* __restrict__ wob, char* __restrict__ out) {
  extern __shared__ __attribute__((aligned(16))) char lds[];
  const int xcd = blockIdx.x & 7, idx = blockIdx.x >> 3, br = xcd >> 2, mb = 4 * (xcd & 3) + (idx >> 3), nb = idx & 7;
  const int tid = threadIdx.x, lane = tid & 63, wid = rfl(tid >> 6), wm = wid >> 1, wn = wid & 1, h = lane >> 5;
  const unsigned lds0 = (unsigned)(uintptr_t)lds;
  const char* xbase = ws + WS_O + ((size_t)br * 64 + mb * 4 + wid) * 65536;
  const char* W = (br ? wob : woa) + nb * 256;
  unsigned wo[4];
#pragma unroll
  for (int i = 0; i < 4; ++i) { const unsigned row = 16 * wid + 4 * i + (lane >> 4); const unsigned g = ((row & 3) << 2) | ((row >> 2) & 3);
    wo[i] = row * 2048u + (((lane & 15) ^ g) << 4); }
  const unsigned lo16 = lane * 16;
  auto stage_issue = [&](int st) {
    const unsigned slot = lds0 + (st & 3) * O_SLOT;
#pragma unroll
    for (int i = 0; i < 4; ++i) glds16(xbase + st * 4096 + i * 1024, lo16, slot + wid * 4096 + i * 1024);
    const char* wb = W + (size_t)st * 64 * 2048;
#pragma unroll
    for (int i = 0; i < 4; ++i) glds16(wb, wo[i], slot + O_XB + wid * 4096 + i * 1024);
  };
  const int blk = (lane >> 4) & 1, q = (lane & 15) >> 2, p = lane & 3;
  unsigned wa[2][2];
#pragma unroll
  for (int c = 0; c < 2; ++c)
#pragma unroll
    for (int t = 0; t < 2; ++t) { const int row = 8 * t + 4 * h + q; const int ch = (4 * (2 * wn + c) + 2 * blk + (p >> 1)) ^ ((q << 2) | ((2 * t + h) & 3));
      wa[c][t] = O_XB + row * 256 + ch * 16 + 8 * (p & 1); }
  const unsigned xa = (2 * wm) * 4096 + lane * 16;
  f32x16 acc[2][2] = {};
  stage_issue(0); stage_issue(1); stage_issue(2);
  for (int st = 0; st < 16; ++st) {
    if (st < 14) WAIT_BAR(16); else if (st < 15) WAIT_BAR(8); else WAIT_BAR(0);
    if (st + 3 < 16) stage_issue(st + 3);
    const lds_cptr sl = (lds_cptr)lds + (st & 3) * O_SLOT;
#pragma unroll
    for (int s = 0; s < 4; ++s) {
      const bf16x8 x0 = lds_b128(sl + xa + s * 1024), x1 = lds_b128(sl + xa + 4096 + s * 1024);
      const bf16x8 w0 = cat8(lds_tr(sl + wa[0][0] + s * 4096), lds_tr(sl + wa[0][1] + s * 4096));
      const bf16x8 w1 = cat8(lds_tr(sl + wa[1][0] + s * 4096), lds_tr(sl + wa[1][1] + s * 4096));
      acc[0][0] = MFMA32(w0, x0, acc[0][0]); acc[0][1] = MFMA32(w0, x1, acc[0][1]);
      acc[1][0] = MFMA32(w1, x0, acc[1][0]); acc[1][1] = MFMA32(w1, x1, acc[1][1]);
    }
  }
#pragma unroll
  for (int c = 0; c < 2; ++c)
#pragma unroll
    for (int tb = 0; tb < 2; ++tb) {
      const int tok = mb * 128 + wm * 64 + tb * 32 + (lane & 31);
      char* o = out + (size_t)tok * 4096 + br * 2048 + nb * 256 + (2 * wn + c) * 64 + h * 8;
#pragma unroll
      for (int g = 0; g < 4; ++g) {
        u32x2 v = {cvtpk(acc[c][tb][4 * g], acc[c][tb][4 * g + 1]), cvtpk(acc[c][tb][4 * g + 2], acc[c][tb][4 * g + 3])};
        *(u32x2*)(o + g * 16) = v;
      }
    }
}

extern "C" void kernel_launch(void* const* d_in, const int* in_sizes, int n_in, void* d_out, int out_size, void* d_ws, size_t ws_size, hipStream_t stream) {
  (void)in_sizes; (void)n_in; (void)out_size; (void)ws_size;
  hipFuncSetAttribute((const void*)k_proj, hipFuncAttributeMaxDynamicSharedMemorySize, P_LDS);
  hipFuncSetAttribute((const void*)k_out, hipFuncAttributeMaxDynamicSharedMemorySize, O_LDS);
  char* ws = (char*)d_ws;
  k_proj<<<256, 512, P_LDS, stream>>>((const char*)d_in[0], (const char*)d_in[1], (const char*)d_in[2], (const char*)d_in[3], (const char*)d_in[5], (const char*)d_in[6], ws);
  k_attn<<<256, 512, 0, stream>>>(ws);
  k_out<<<256, 256, O_LDS, stream>>>(ws, (const char*)d_in[4], (const char*)d_in[7], (char*)d_out);
}
```

```cpp
#include <hip/hip_runtime.h>
#include <hip/hip_bf16.h>
#include <stdint.h>

typedef short    s16x4  __attribute__((ext_vector_type(4)));
typedef short    bf16x8 __attribute__((ext_vector_type(8)));
typedef __bf16   bf16x2 __attribute__((ext_vector_type(2)));
typedef float    f32x2  __attribute__((ext_vector_type(2)));
typedef float    f32x4  __attribute__((ext_vector_type(4)));
typedef float    f32x16 __attribute__((ext_vector_type(16)));
typedef unsigned u32x4  __attribute__((ext_vector_type(4)));
typedef unsigned u32x2  __attribute__((ext_vector_type(2)));
#define DI __device__ __forceinline__
#define MFMA32(a, b, c) __builtin_amdgcn_mfma_f32_32x32x16_bf16((a), (b), (c), 0, 0, 0)

constexpr float QSCALE = 0.17677669529663687f * 1.4426950408889634f;

constexpr size_t WS_QA  = 0;
constexpr size_t WS_QB  = WS_QA + (4u << 20);
constexpr size_t WS_KVA = WS_QB + (4u << 20);
constexpr size_t WS_KVB = WS_KVA + (256u << 10);
constexpr size_t WS_O   = WS_KVB + (8u << 20);

DI unsigned cvtpk(float lo, float hi) { f32x2 v = {lo, hi}; bf16x2 b = __builtin_convertvector(v, bf16x2); return __builtin_bit_cast(unsigned, b); }
DI u32x4 pack8(const f32x16& x, int s, float sc) {
  u32x4 p;
  p[0] = cvtpk(x[8 * s + 0] * sc, x[8 * s + 1] * sc); p[1] = cvtpk(x[8 * s + 2] * sc, x[8 * s + 3] * sc);
  p[2] = cvtpk(x[8 * s + 4] * sc, x[8 * s + 5] * sc); p[3] = cvtpk(x[8 * s + 6] * sc, x[8 * s + 7] * sc);
  return p;
}
DI void glds16(const void* sbase, unsigned voff, unsigned lds_base) {
  unsigned sv;
  asm volatile("s_mov_b32 %0, m0\n\ts_mov_b32 m0, %3\n\ts_nop 0\n\tglobal_load_lds_dwordx4 %1, %2\n\ts_mov_b32 m0, %0"
               : "=&s"(sv) : "v"(voff), "s"(sbase), "s"(lds_base) : "memory");
}
DI void st16(void* p, u32x4 v) { asm volatile("global_store_dwordx4 %0, %1, off sc1\n\ts_nop 1" :: "v"(p), "v"(v) : "memory"); }
DI unsigned rfl(unsigned x) { return __builtin_amdgcn_readfirstlane(x); }
template <int N> DI void waitv() { asm volatile("s_waitcnt vmcnt(%0)" :: "i"(N) : "memory"); }
DI void bar_l() { asm volatile("s_waitcnt lgkmcnt(0)\n\ts_barrier" ::: "memory"); }

DI void proj_core_wx(f32x16* acc, unsigned* xa, unsigned* wa) {
  asm volatile(
    "s_mov_b32 s60, 16\n\t"
    "s_mov_b32 s61, 0\n\t"
    "s_mov_b32 s63, 49152\n\t"
    "s_mov_b32 s64, 4294868992\n\t"
    "s_barrier\n\t"
    "ds_read_b64_tr_b16 v[176:177], v210 offset:32768\n\t"
    "ds_read_b64_tr_b16 v[178:179], v211 offset:32768\n\t"
    "ds_read_b64_tr_b16 v[180:181], v212 offset:32768\n\t"
    "ds_read_b64_tr_b16 v[182:183], v213 offset:32768\n\t"
    "ds_read_b64_tr_b16 v[184:185], v214 offset:32768\n\t"
    "ds_read_b64_tr_b16 v[186:187], v215 offset:32768\n\t"
    "ds_read_b64_tr_b16 v[188:189], v216 offset:32768\n\t"
    "ds_read_b64_tr_b16 v[190:191], v217 offset:32768\n\t"
    "ds_read_b128 v[144:147], v208 offset:0\n\t"
    "ds_read_b128 v[148:151], v208 offset:2048\n\t"
    "ds_read_b128 v[152:155], v208 offset:4096\n\t"
    "ds_read_b128 v[156:159], v208 offset:6144\n\t"
    "ds_read_b128 v[160:163], v208 offset:8192\n\t"
    "ds_read_b128 v[164:167], v208 offset:10240\n\t"
    "ds_read_b128 v[168:171], v208 offset:12288\n\t"
    "ds_read_b128 v[172:175], v208 offset:14336\n\t"
    "LOOP_%=:\n\t"
    "s_waitcnt lgkmcnt(7)\n\t"
    "v_mfma_f32_16x16x32_bf16 v[16:19], v[176:179], v[144:147], v[16:19]\n\t"
    "ds_read_b64_tr_b16 v[192:193], v210 offset:40960\n\t"
    "v_mfma_f32_16x16x32_bf16 v[20:23], v[180:183], v[144:147], v[20:23]\n\t"
    "ds_read_b64_tr_b16 v[194:195], v211 offset:40960\n\t"
    "v_mfma_f32_16x16x32_bf16 v[24:27], v[184:187], v[144:147], v[24:27]\n\t"
    "ds_read_b64_tr_b16 v[196:197], v212 offset:40960\n\t"
    "v_mfma_f32_16x16x32_bf16 v[28:31], v[188:191], v[144:147], v[28:31]\n\t"
    "ds_read_b64_tr_b16 v[198:199], v213 offset:40960\n\t"
    "s_waitcnt lgkmcnt(10)\n\t"
    "v_mfma_f32_16x16x32_bf16 v[32:35], v[176:179], v[148:151], v[32:35]\n\t"
    "ds_read_b64_tr_b16 v[200:201], v214 offset:40960\n\t"
    "v_mfma_f32_16x16x32_bf16 v[36:39], v[180:183], v[148:151], v[36:39]\n\t"
    "ds_read_b64_tr_b16 v[202:203], v215 offset:40960\n\t"
    "v_mfma_f32_16x16x32_bf16 v[40:43], v[184:187], v[148:151], v[40:43]\n\t"
    "ds_read_b64_tr_b16 v[204:205], v216 offset:40960\n\t"
    "v_mfma_f32_16x16x32_bf16 v[44:47], v[188:191], v[148:151], v[44:47]\n\t"
    "ds_read_b64_tr_b16 v[206:207], v217 offset:40960\n\t"
    "s_waitcnt lgkmcnt(13)\n\t"
    "v_mfma_f32_16x16x32_bf16 v[48:51], v[176:179], v[152:155], v[48:51]\n\t"
    "ds_read_b128 v[144:147], v209 offset:0\n\t"
    "v_mfma_f32_16x16x32_bf16 v[52:55], v[180:183], v[152:155], v[52:55]\n\t"
    "ds_read_b128 v[148:151], v209 offset:2048\n\t"
    "v_mfma_f32_16x16x32_bf16 v[56:59], v[184:187], v[152:155], v[56:59]\n\t"
    "v_mfma_f32_16x16x32_bf16 v[60:63], v[188:191], v[152:155], v[60:63]\n\t"
    "ds_read_b128 v[152:155], v209 offset:4096\n\t"
    "s_waitcnt lgkmcnt(15)\n\t"
    "v_mfma_f32_16x16x32_bf16 v[64:67], v[176:179], v[156:159], v[64:67]\n\t"
    "v_mfma_f32_16x16x32_bf16 v[68:71], v[180:183], v[156:159], v[68:71]\n\t"
    "v_mfma_f32_16x16x32_bf16 v[72:75], v[184:187], v[156:159], v[72:75]\n\t"
    "v_mfma_f32_16x16x32_bf16 v[76:79], v[188:191], v[156:159], v[76:79]\n\t"
    "ds_read_b128 v[156:159], v209 offset:6144\n\t"
    "s_waitcnt lgkmcnt(15)\n\t"
    "v_mfma_f32_16x16x32_bf16 v[80:83], v[176:179], v[160:163], v[80:83]\n\t"
    "v_mfma_f32_16x16x32_bf16 v[84:87], v[180:183], v[160:163], v[84:87]\n\t"
    "v_mfma_f32_16x16x32_bf16 v[88:91], v[184:187], v[160:163], v[88:91]\n\t"
    "v_mfma_f32_16x16x32_bf16 v[92:95], v[188:191], v[160:163], v[92:95]\n\t"
    "ds_read_b128 v[160:163], v209 offset:8192\n\t"
    "s_waitcnt lgkmcnt(15)\n\t"
    "v_mfma_f32_16x16x32_bf16 v[96:99], v[176:179], v[164:167], v[96:99]\n\t"
    "v_mfma_f32_16x16x32_bf16 v[100:103], v[180:183], v[164:167], v[100:103]\n\t"
    "v_mfma_f32_16x16x32_bf16 v[104:107], v[184:187], v[164:167], v[104:107]\n\t"
    "v_mfma_f32_16x16x32_bf16 v[108:111], v[188:191], v[164:167], v[108:111]\n\t"
    "ds_read_b128 v[164:167], v209 offset:10240\n\t"
    "s_waitcnt lgkmcnt(15)\n\t"
    "v_mfma_f32_16x16x32_bf16 v[112:115], v[176:179], v[168:171], v[112:115]\n\t"
    "v_mfma_f32_16x16x32_bf16 v[116:119], v[180:183], v[168:171], v[116:119]\n\t"
    "v_mfma_f32_16x16x32_bf16 v[120:123], v[184:187], v[168:171], v[120:123]\n\t"
    "v_mfma_f32_16x16x32_bf16 v[124:127], v[188:191], v[168:171], v[124:127]\n\t"
    "ds_read_b128 v[168:171], v209 offset:12288\n\t"
    "s_waitcnt lgkmcnt(15)\n\t"
    "v_mfma_f32_16x16x32_bf16 v[128:131], v[176:179], v[172:175], v[128:131]\n\t"
    "v_mfma_f32_16x16x32_bf16 v[132:135], v[180:183], v[172:175], v[132:135]\n\t"
    "v_mfma_f32_16x16x32_bf16 v[136:139], v[184:187], v[172:175], v[136:139]\n\t"
    "v_mfma_f32_16x16x32_bf16 v[140:143], v[188:191], v[172:175], v[140:143]\n\t"
    "ds_read_b128 v[172:175], v209 offset:14336\n\t"
    "s_waitcnt lgkmcnt(7)\n\t"
    "v_mfma_f32_16x16x32_bf16 v[16:19], v[192:195], v[144:147], v[16:19]\n\t"
    "v_mfma_f32_16x16x32_bf16 v[20:23], v[196:199], v[144:147], v[20:23]\n\t"
    "v_mfma_f32_16x16x32_bf16 v[24:27], v[200:203], v[144:147], v[24:27]\n\t"
    "v_mfma_f32_16x16x32_bf16 v[28:31], v[204:207], v[144:147], v[28:31]\n\t"
    "s_waitcnt lgkmcnt(6)\n\t"
    "v_mfma_f32_16x16x32_bf16 v[32:35], v[192:195], v[148:151], v[32:35]\n\t"
    "v_mfma_f32_16x16x32_bf16 v[36:39], v[196:199], v[148:151], v[36:39]\n\t"
    "v_mfma_f32_16x16x32_bf16 v[40:43], v[200:203], v[148:151], v[40:43]\n\t"
    "v_mfma_f32_16x16x32_bf16 v[44:47], v[204:207], v[148:151], v[44:47]\n\t"
    "s_waitcnt lgkmcnt(5)\n\t"
    "v_mfma_f32_16x16x32_bf16 v[48:51], v[192:195], v[152:155], v[48:51]\n\t"
    "v_mfma_f32_16x16x32_bf16 v[52:55], v[196:199], v[152:155], v[52:55]\n\t"
    "v_mfma_f32_16x16x32_bf16 v[56:59], v[200:203], v[152:155], v[56:59]\n\t"
    "v_mfma_f32_16x16x32_bf16 v[60:63], v[204:207], v[152:155], v[60:63]\n\t"
    "s_waitcnt lgkmcnt(0)\n\t"
    "s_barrier\n\t"
    "s_add_u32 s61, s61, 1\n\t"
    "s_cmp_eq_u32 s61, 3\n\t"
    "s_cselect_b32 s62, s64, s63\n\t"
    "s_cselect_b32 s61, 0, s61\n\t"
    "v_add_u32 v208, s62, v208\n\t"
    "v_add_u32 v209, s62, v209\n\t"
    "v_add_u32 v210, s62, v210\n\t"
    "v_add_u32 v211, s62, v211\n\t"
    "v_add_u32 v212, s62, v212\n\t"
    "v_add_u32 v213, s62, v213\n\t"
    "v_add_u32 v214, s62, v214\n\t"
    "v_add_u32 v215, s62, v215\n\t"
    "v_add_u32 v216, s62, v216\n\t"
    "v_add_u32 v217, s62, v217\n\t"
    "ds_read_b64_tr_b16 v[176:177], v210 offset:32768\n\t"
    "v_mfma_f32_16x16x32_bf16 v[64:67], v[192:195], v[156:159], v[64:67]\n\t"
    "ds_read_b64_tr_b16 v[178:179], v211 offset:32768\n\t"
    "v_mfma_f32_16x16x32_bf16 v[68:71], v[196:199], v[156:159], v[68:71]\n\t"
    "ds_read_b64_tr_b16 v[180:181], v212 offset:32768\n\t"
    "v_mfma_f32_16x16x32_bf16 v[72:75], v[200:203], v[156:159], v[72:75]\n\t"
    "ds_read_b64_tr_b16 v[182:183], v213 offset:32768\n\t"
    "v_mfma_f32_16x16x32_bf16 v[76:79], v[204:207], v[156:159], v[76:79]\n\t"
    "ds_read_b64_tr_b16 v[184:185], v214 offset:32768\n\t"
    "v_mfma_f32_16x16x32_bf16 v[80:83], v[192:195], v[160:163], v[80:83]\n\t"
    "ds_read_b64_tr_b16 v[186:187], v215 offset:32768\n\t"
    "v_mfma_f32_16x16x32_bf16 v[84:87], v[196:199], v[160:163], v[84:87]\n\t"
    "ds_read_b64_tr_b16 v[188:189], v216 offset:32768\n\t"
    "v_mfma_f32_16x16x32_bf16 v[88:91], v[200:203], v[160:163], v[88:91]\n\t"
    "ds_read_b64_tr_b16 v[190:191], v217 offset:32768\n\t"
    "v_mfma_f32_16x16x32_bf16 v[92:95], v[204:207], v[160:163], v[92:95]\n\t"
    "ds_read_b128 v[144:147], v208 offset:0\n\t"
    "v_mfma_f32_16x16x32_bf16 v[96:99], v[192:195], v[164:167], v[96:99]\n\t"
    "ds_read_b128 v[148:151], v208 offset:2048\n\t"
    "v_mfma_f32_16x16x32_bf16 v[100:103], v[196:199], v[164:167], v[100:103]\n\t"
    "ds_read_b128 v[152:155], v208 offset:4096\n\t"
    "v_mfma_f32_16x16x32_bf16 v[104:107], v[200:203], v[164:167], v[104:107]\n\t"
    "ds_read_b128 v[156:159], v208 offset:6144\n\t"
    "v_mfma_f32_16x16x32_bf16 v[108:111], v[204:207], v[164:167], v[108:111]\n\t"
    "ds_read_b128 v[160:163], v208 offset:8192\n\t"
    "v_mfma_f32_16x16x32_bf16 v[112:115], v[192:195], v[168:171], v[112:115]\n\t"
    "ds_read_b128 v[164:167], v208 offset:10240\n\t"
    "v_mfma_f32_16x16x32_bf16 v[116:119], v[196:199], v[168:171], v[116:119]\n\t"
    "v_mfma_f32_16x16x32_bf16 v[120:123], v[200:203], v[168:171], v[120:123]\n\t"
    "v_mfma_f32_16x16x32_bf16 v[124:127], v[204:207], v[168:171], v[124:127]\n\t"
    "ds_read_b128 v[168:171], v208 offset:12288\n\t"
    "v_mfma_f32_16x16x32_bf16 v[128:131], v[192:195], v[172:175], v[128:131]\n\t"
    "v_mfma_f32_16x16x32_bf16 v[132:135], v[196:199], v[172:175], v[132:135]\n\t"
    "v_mfma_f32_16x16x32_bf16 v[136:139], v[200:203], v[172:175], v[136:139]\n\t"
    "v_mfma_f32_16x16x32_bf16 v[140:143], v[204:207], v[172:175], v[140:143]\n\t"
    "ds_read_b128 v[172:175], v208 offset:14336\n\t"
    "s_sub_u32 s60, s60, 1\n\t"
    "s_cmp_lg_u32 s60, 0\n\t"
    "s_cbranch_scc1 LOOP_%=\n\t"
    "s_waitcnt lgkmcnt(0)\n\t"
    "s_nop 7\n\t"
    "s_nop 3\n\t"
    : "+{v[16:31]}"(acc[0]), "+{v[32:47]}"(acc[1]), "+{v[48:63]}"(acc[2]), "+{v[64:79]}"(acc[3]), "+{v[80:95]}"(acc[4]), "+{v[96:111]}"(acc[5]), "+{v[112:127]}"(acc[6]), "+{v[128:143]}"(acc[7]), "+{v208}"(xa[0]), "+{v209}"(xa[1]), "+{v210}"(wa[0]), "+{v211}"(wa[1]), "+{v212}"(wa[2]), "+{v213}"(wa[3]), "+{v214}"(wa[4]), "+{v215}"(wa[5]), "+{v216}"(wa[6]), "+{v217}"(wa[7])
    :
    : "memory", "scc", "v144", "v145", "v146", "v147", "v148", "v149", "v150", "v151", "v152", "v153", "v154", "v155", "v156", "v157", "v158", "v159", "v160", "v161", "v162", "v163", "v164", "v165", "v166", "v167", "v168", "v169", "v170", "v171", "v172", "v173", "v174", "v175", "v176", "v177", "v178", "v179", "v180", "v181", "v182", "v183", "v184", "v185", "v186", "v187", "v188", "v189", "v190", "v191", "v192", "v193", "v194", "v195", "v196", "v197", "v198", "v199", "v200", "v201", "v202", "v203", "v204", "v205", "v206", "v207", "s60", "s61", "s62", "s63", "s64", "s65", "s66", "s67");
}

DI void proj_core_xw(f32x16* acc, unsigned* xa, unsigned* wa) {
  asm volatile(
    "s_mov_b32 s60, 16\n\t"
    "s_mov_b32 s61, 0\n\t"
    "s_mov_b32 s63, 49152\n\t"
    "s_mov_b32 s64, 4294868992\n\t"
    "s_barrier\n\t"
    "ds_read_b64_tr_b16 v[176:177], v210 offset:32768\n\t"
    "ds_read_b64_tr_b16 v[178:179], v211 offset:32768\n\t"
    "ds_read_b64_tr_b16 v[180:181], v212 offset:32768\n\t"
    "ds_read_b64_tr_b16 v[182:183], v213 offset:32768\n\t"
    "ds_read_b64_tr_b16 v[184:185], v214 offset:32768\n\t"
    "ds_read_b64_tr_b16 v[186:187], v215 offset:32768\n\t"
    "ds_read_b64_tr_b16 v[188:189], v216 offset:32768\n\t"
    "ds_read_b64_tr_b16 v[190:191], v217 offset:32768\n\t"
    "ds_read_b128 v[144:147], v208 offset:0\n\t"
    "ds_read_b128 v[148:151], v208 offset:2048\n\t"
    "ds_read_b128 v[152:155], v208 offset:4096\n\t"
    "ds_read_b128 v[156:159], v208 offset:6144\n\t"
    "ds_read_b128 v[160:163], v208 offset:8192\n\t"
    "ds_read_b128 v[164:167], v208 offset:10240\n\t"
    "ds_read_b128 v[168:171], v208 offset:12288\n\t"
    "ds_read_b128 v[172:175], v208 offset:14336\n\t"
    "LOOP_%=:\n\t"
    "s_waitcnt lgkmcnt(7)\n\t"
    "v_mfma_f32_16x16x32_bf16 v[16:19], v[144:147], v[176:179], v[16:19]\n\t"
    "ds_read_b64_tr_b16 v[192:193], v210 offset:40960\n\t"
    "v_mfma_f32_16x16x32_bf16 v[20:23], v[144:147], v[180:183], v[20:23]\n\t"
    "ds_read_b64_tr_b16 v[194:195], v211 offset:40960\n\t"
    "v_mfma_f32_16x16x32_bf16 v[24:27], v[144:147], v[184:187], v[24:27]\n\t"
    "ds_read_b64_tr_b16 v[196:197], v212 offset:40960\n\t"
    "v_mfma_f32_16x16x32_bf16 v[28:31], v[144:147], v[188:191], v[28:31]\n\t"
    "ds_read_b64_tr_b16 v[198:199], v213 offset:40960\n\t"
    "s_waitcnt lgkmcnt(10)\n\t"
    "v_mfma_f32_16x16x32_bf16 v[32:35], v[148:151], v[176:179], v[32:35]\n\t"
    "ds_read_b64_tr_b16 v[200:201], v214 offset:40960\n\t"
    "v_mfma_f32_16x16x32_bf16 v[36:39], v[148:151], v[180:183], v[36:39]\n\t"
    "ds_read_b64_tr_b16 v[202:203], v215 offset:40960\n\t"
    "v_mfma_f32_16x16x32_bf16 v[40:43], v[148:151], v[184:187], v[40:43]\n\t"
    "ds_read_b64_tr_b16 v[204:205], v216 offset:40960\n\t"
    "v_mfma_f32_16x16x32_bf16 v[44:47], v[148:151], v[188:191], v[44:47]\n\t"
    "ds_read_b64_tr_b16 v[206:207], v217 offset:40960\n\t"
    "s_waitcnt lgkmcnt(13)\n\t"
    "v_mfma_f32_16x16x32_bf16 v[48:51], v[152:155], v[176:179], v[48:51]\n\t"
    "ds_read_b128 v[144:147], v209 offset:0\n\t"
    "v_mfma_f32_16x16x32_bf16 v[52:55], v[152:155], v[180:183], v[52:55]\n\t"
    "ds_read_b128 v[148:151], v209 offset:2048\n\t"
    "v_mfma_f32_16x16x32_bf16 v[56:59], v[152:155], v[184:187], v[56:59]\n\t"
    "v_mfma_f32_16x16x32_bf16 v[60:63], v[152:155], v[188:191], v[60:63]\n\t"
    "ds_read_b128 v[152:155], v209 offset:4096\n\t"
    "s_waitcnt lgkmcnt(15)\n\t"
    "v_mfma_f32_16x16x32_bf16 v[64:67], v[156:159], v[176:179], v[64:67]\n\t"
    "v_mfma_f32_16x16x32_bf16 v[68:71], v[156:159], v[180:183], v[68:71]\n\t"
    "v_mfma_f32_16x16x32_bf16 v[72:75], v[156:159], v[184:187], v[72:75]\n\t"
    "v_mfma_f32_16x16x32_bf16 v[76:79], v[156:159], v[188:191], v[76:79]\n\t"
    "ds_read_b128 v[156:159], v209 offset:6144\n\t"
    "s_waitcnt lgkmcnt(15)\n\t"
    "v_mfma_f32_16x16x32_bf16 v[80:83], v[160:163], v[176:179], v[80:83]\n\t"
    "v_mfma_f32_16x16x32_bf16 v[84:87], v[160:163], v[180:183], v[84:87]\n\t"
    "v_mfma_f32_16x16x32_bf16 v[88:91], v[160:163], v[184:187], v[88:91]\n\t"
    "v_mfma_f32_16x16x32_bf16 v[92:95], v[160:163], v[188:191], v[92:95]\n\t"
    "ds_read_b128 v[160:163], v209 offset:8192\n\t"
    "s_waitcnt lgkmcnt(15)\n\t"
    "v_mfma_f32_16x16x32_bf16 v[96:99], v[164:167], v[176:179], v[96:99]\n\t"
    "v_mfma_f32_16x16x32_bf16 v[100:103], v[164:167], v[180:183], v[100:103]\n\t"
    "v_mfma_f32_16x16x32_bf16 v[104:107], v[164:167], v[184:187], v[104:107]\n\t"
    "v_mfma_f32_16x16x32_bf16 v[108:111], v[164:167], v[188:191], v[108:111]\n\t"
    "ds_read_b128 v[164:167], v209 offset:10240\n\t"
    "s_waitcnt lgkmcnt(15)\n\t"
    "v_mfma_f32_16x16x32_bf16 v[112:115], v[168:171], v[176:179], v[112:115]\n\t"
    "v_mfma_f32_16x16x32_bf16 v[116:119], v[168:171], v[180:183], v[116:119]\n\t"
    "v_mfma_f32_16x16x32_bf16 v[120:123], v[168:171], v[184:187], v[120:123]\n\t"
    "v_mfma_f32_16x16x32_bf16 v[124:127], v[168:171], v[188:191], v[124:127]\n\t"
    "ds_read_b128 v[168:171], v209 offset:12288\n\t"
    "s_waitcnt lgkmcnt(15)\n\t"
    "v_mfma_f32_16x16x32_bf16 v[128:131], v[172:175], v[176:179], v[128:131]\n\t"
    "v_mfma_f32_16x16x32_bf16 v[132:135], v[172:175], v[180:183], v[132:135]\n\t"
    "v_mfma_f32_16x16x32_bf16 v[136:139], v[172:175], v[184:187], v[136:139]\n\t"
    "v_mfma_f32_16x16x32_bf16 v[140:143], v[172:175], v[188:191], v[140:143]\n\t"
    "ds_read_b128 v[172:175], v209 offset:14336\n\t"
    "s_waitcnt lgkmcnt(7)\n\t"
    "v_mfma_f32_16x16x32_bf16 v[16:19], v[144:147], v[192:195], v[16:19]\n\t"
    "v_mfma_f32_16x16x32_bf16 v[20:23], v[144:147], v[196:199], v[20:23]\n\t"
    "v_mfma_f32_16x16x32_bf16 v[24:27], v[144:147], v[200:203], v[24:27]\n\t"
    "v_mfma_f32_16x16x32_bf16 v[28:31], v[144:147], v[204:207], v[28:31]\n\t"
    "s_waitcnt lgkmcnt(6)\n\t"
    "v_mfma_f32_16x16x32_bf16 v[32:35], v[148:151], v[192:195], v[32:35]\n\t"
    "v_mfma_f32_16x16x32_bf16 v[36:39], v[148:151], v[196:199], v[36:39]\n\t"
    "v_mfma_f32_16x16x32_bf16 v[40:43], v[148:151], v[200:203], v[40:43]\n\t"
    "v_mfma_f32_16x16x32_bf16 v[44:47], v[148:151], v[204:207], v[44:47]\n\t"
    "s_waitcnt lgkmcnt(5)\n\t"
    "v_mfma_f32_16x16x32_bf16 v[48:51], v[152:155], v[192:195], v[48:51]\n\t"
    "v_mfma_f32_16x16x32_bf16 v[52:55], v[152:155], v[196:199], v[52:55]\n\t"
    "v_mfma_f32_16x16x32_bf16 v[56:59], v[152:155], v[200:203], v[56:59]\n\t"
    "v_mfma_f32_16x16x32_bf16 v[60:63], v[152:155], v[204:207], v[60:63]\n\t"
    "s_waitcnt lgkmcnt(0)\n\t"
    "s_barrier\n\t"
    "s_add_u32 s61, s61, 1\n\t"
    "s_cmp_eq_u32 s61, 3\n\t"
    "s_cselect_b32 s62, s64, s63\n\t"
    "s_cselect_b32 s61, 0, s61\n\t"
    "v_add_u32 v208, s62, v208\n\t"
    "v_add_u32 v209, s62, v209\n\t"
    "v_add_u32 v210, s62, v210\n\t"
    "v_add_u32 v211, s62, v211\n\t"
    "v_add_u32 v212, s62, v212\n\t"
    "v_add_u32 v213, s62, v213\n\t"
    "v_add_u32 v214, s62, v214\n\t"
    "v_add_u32 v215, s62, v215\n\t"
    "v_add_u32 v216, s62, v216\n\t"
    "v_add_u32 v217, s62, v217\n\t"
    "ds_read_b64_tr_b16 v[176:177], v210 offset:32768\n\t"
    "v_mfma_f32_16x16x32_bf16 v[64:67], v[156:159], v[192:195], v[64:67]\n\t"
    "ds_read_b64_tr_b16 v[178:179], v211 offset:32768\n\t"
    "v_mfma_f32_16x16x32_bf16 v[68:71], v[156:159], v[196:199], v[68:71]\n\t"
    "ds_read_b64_tr_b16 v[180:181], v212 offset:32768\n\t"
    "v_mfma_f32_16x16x32_bf16 v[72:75], v[156:159], v[200:203], v[72:75]\n\t"
    "ds_read_b64_tr_b16 v[182:183], v213 offset:32768\n\t"
    "v_mfma_f32_16x16x32_bf16 v[76:79], v[156:159], v[204:207], v[76:79]\n\t"
    "ds_read_b64_tr_b16 v[184:185], v214 offset:32768\n\t"
    "v_mfma_f32_16x16x32_bf16 v[80:83], v[160:163], v[192:195], v[80:83]\n\t"
    "ds_read_b64_tr_b16 v[186:187], v215 offset:32768\n\t"
    "v_mfma_f32_16x16x32_bf16 v[84:87], v[160:163], v[196:199], v[84:87]\n\t"
    "ds_read_b64_tr_b16 v[188:189], v216 offset:32768\n\t"
    "v_mfma_f32_16x16x32_bf16 v[88:91], v[160:163], v[200:203], v[88:91]\n\t"
    "ds_read_b64_tr_b16 v[190:191], v217 offset:32768\n\t"
    "v_mfma_f32_16x16x32_bf16 v[92:95], v[160:163], v[204:207], v[92:95]\n\t"
    "ds_read_b128 v[144:147], v208 offset:0\n\t"
    "v_mfma_f32_16x16x32_bf16 v[96:99], v[164:167], v[192:195], v[96:99]\n\t"
    "ds_read_b128 v[148:151], v208 offset:2048\n\t"
    "v_mfma_f32_16x16x32_bf16 v[100:103], v[164:167], v[196:199], v[100:103]\n\t"
    "ds_read_b128 v[152:155], v208 offset:4096\n\t"
    "v_mfma_f32_16x16x32_bf16 v[104:107], v[164:167], v[200:203], v[104:107]\n\t"
    "ds_read_b128 v[156:159], v208 offset:6144\n\t"
    "v_mfma_f32_16x16x32_bf16 v[108:111], v[164:167], v[204:207], v[108:111]\n\t"
    "ds_read_b128 v[160:163], v208 offset:8192\n\t"
    "v_mfma_f32_16x16x32_bf16 v[112:115], v[168:171], v[192:195], v[112:115]\n\t"
    "ds_read_b128 v[164:167], v208 offset:10240\n\t"
    "v_mfma_f32_16x16x32_bf16 v[116:119], v[168:171], v[196:199], v[116:119]\n\t"
    "v_mfma_f32_16x16x32_bf16 v[120:123], v[168:171], v[200:203], v[120:123]\n\t"
    "v_mfma_f32_16x16x32_bf16 v[124:127], v[168:171], v[204:207], v[124:127]\n\t"
    "ds_read_b128 v[168:171], v208 offset:12288\n\t"
    "v_mfma_f32_16x16x32_bf16 v[128:131], v[172:175], v[192:195], v[128:131]\n\t"
    "v_mfma_f32_16x16x32_bf16 v[132:135], v[172:175], v[196:199], v[132:135]\n\t"
    "v_mfma_f32_16x16x32_bf16 v[136:139], v[172:175], v[200:203], v[136:139]\n\t"
    "v_mfma_f32_16x16x32_bf16 v[140:143], v[172:175], v[204:207], v[140:143]\n\t"
    "ds_read_b128 v[172:175], v208 offset:14336\n\t"
    "s_sub_u32 s60, s60, 1\n\t"
    "s_cmp_lg_u32 s60, 0\n\t"
    "s_cbranch_scc1 LOOP_%=\n\t"
    "s_waitcnt lgkmcnt(0)\n\t"
    "s_nop 7\n\t"
    "s_nop 3\n\t"
    : "+{v[16:31]}"(acc[0]), "+{v[32:47]}"(acc[1]), "+{v[48:63]}"(acc[2]), "+{v[64:79]}"(acc[3]), "+{v[80:95]}"(acc[4]), "+{v[96:111]}"(acc[5]), "+{v[112:127]}"(acc[6]), "+{v[128:143]}"(acc[7]), "+{v208}"(xa[0]), "+{v209}"(xa[1]), "+{v210}"(wa[0]), "+{v211}"(wa[1]), "+{v212}"(wa[2]), "+{v213}"(wa[3]), "+{v214}"(wa[4]), "+{v215}"(wa[5]), "+{v216}"(wa[6]), "+{v217}"(wa[7])
    :
    : "memory", "scc", "v144", "v145", "v146", "v147", "v148", "v149", "v150", "v151", "v152", "v153", "v154", "v155", "v156", "v157", "v158", "v159", "v160", "v161", "v162", "v163", "v164", "v165", "v166", "v167", "v168", "v169", "v170", "v171", "v172", "v173", "v174", "v175", "v176", "v177", "v178", "v179", "v180", "v181", "v182", "v183", "v184", "v185", "v186", "v187", "v188", "v189", "v190", "v191", "v192", "v193", "v194", "v195", "v196", "v197", "v198", "v199", "v200", "v201", "v202", "v203", "v204", "v205", "v206", "v207", "s60", "s61", "s62", "s63", "s64", "s65", "s66", "s67");
}

DI void proj_core_wx_k(f32x16* acc, unsigned* xa, unsigned* wa, unsigned* ea) {
  asm volatile(
    "s_mov_b32 s66, 4096\n\t"
    "s_mov_b32 s67, 4294959104\n\t"
    "s_mov_b32 s60, 16\n\t"
    "s_mov_b32 s61, 0\n\t"
    "s_mov_b32 s63, 49152\n\t"
    "s_mov_b32 s64, 4294868992\n\t"
    "s_barrier\n\t"
    "ds_read_b64_tr_b16 v[192:193], v242 offset:32768\n\t"
    "ds_read_b64_tr_b16 v[194:195], v243 offset:32768\n\t"
    "ds_read_b64_tr_b16 v[196:197], v244 offset:32768\n\t"
    "ds_read_b64_tr_b16 v[198:199], v245 offset:32768\n\t"
    "ds_read_b64_tr_b16 v[200:201], v246 offset:32768\n\t"
    "ds_read_b64_tr_b16 v[202:203], v247 offset:32768\n\t"
    "ds_read_b64_tr_b16 v[204:205], v248 offset:32768\n\t"
    "ds_read_b64_tr_b16 v[206:207], v249 offset:32768\n\t"
    "ds_read_b128 v[160:163], v240 offset:0\n\t"
    "ds_read_b128 v[164:167], v240 offset:2048\n\t"
    "ds_read_b128 v[168:171], v240 offset:4096\n\t"
    "ds_read_b128 v[172:175], v240 offset:6144\n\t"
    "ds_read_b128 v[176:179], v240 offset:8192\n\t"
    "ds_read_b128 v[180:183], v240 offset:10240\n\t"
    "ds_read_b128 v[184:187], v240 offset:12288\n\t"
    "ds_read_b128 v[188:191], v240 offset:14336\n\t"
    "LOOP_%=:\n\t"
    "s_waitcnt lgkmcnt(7)\n\t"
    "v_mfma_f32_16x16x32_bf16 v[16:19], v[192:195], v[160:163], v[16:19]\n\t"
    "ds_read_b64_tr_b16 v[208:209], v242 offset:40960\n\t"
    "v_mfma_f32_16x16x32_bf16 v[20:23], v[196:199], v[160:163], v[20:23]\n\t"
    "ds_read_b64_tr_b16 v[210:211], v243 offset:40960\n\t"
    "v_mfma_f32_16x16x32_bf16 v[24:27], v[200:203], v[160:163], v[24:27]\n\t"
    "ds_read_b64_tr_b16 v[212:213], v244 offset:40960\n\t"
    "v_mfma_f32_16x16x32_bf16 v[28:31], v[204:207], v[160:163], v[28:31]\n\t"
    "ds_read_b64_tr_b16 v[214:215], v245 offset:40960\n\t"
    "s_waitcnt lgkmcnt(10)\n\t"
    "v_mfma_f32_16x16x32_bf16 v[32:35], v[192:195], v[164:167], v[32:35]\n\t"
    "ds_read_b64_tr_b16 v[216:217], v246 offset:40960\n\t"
    "v_mfma_f32_16x16x32_bf16 v[36:39], v[196:199], v[164:167], v[36:39]\n\t"
    "ds_read_b64_tr_b16 v[218:219], v247 offset:40960\n\t"
    "v_mfma_f32_16x16x32_bf16 v[40:43], v[200:203], v[164:167], v[40:43]\n\t"
    "ds_read_b64_tr_b16 v[220:221], v248 offset:40960\n\t"
    "v_mfma_f32_16x16x32_bf16 v[44:47], v[204:207], v[164:167], v[44:47]\n\t"
    "ds_read_b64_tr_b16 v[222:223], v249 offset:40960\n\t"
    "s_waitcnt lgkmcnt(13)\n\t"
    "v_mfma_f32_16x16x32_bf16 v[48:51], v[192:195], v[168:171], v[48:51]\n\t"
    "ds_read_b128 v[160:163], v241 offset:0\n\t"
    "v_mfma_f32_16x16x32_bf16 v[52:55], v[196:199], v[168:171], v[52:55]\n\t"
    "ds_read_b128 v[164:167], v241 offset:2048\n\t"
    "v_mfma_f32_16x16x32_bf16 v[56:59], v[200:203], v[168:171], v[56:59]\n\t"
    "ds_read_b128 v[224:227], v250 offset:0\n\t"
    "v_mfma_f32_16x16x32_bf16 v[60:63], v[204:207], v[168:171], v[60:63]\n\t"
    "ds_read_b128 v[168:171], v241 offset:4096\n\t"
    "s_waitcnt lgkmcnt(15)\n\t"
    "v_mfma_f32_16x16x32_bf16 v[64:67], v[192:195], v[172:175], v[64:67]\n\t"
    "ds_read_b128 v[228:231], v250 offset:2048\n\t"
    "v_mfma_f32_16x16x32_bf16 v[68:71], v[196:199], v[172:175], v[68:71]\n\t"
    "ds_read_b64_tr_b16 v[232:233], v251 offset:0\n\t"
    "v_mfma_f32_16x16x32_bf16 v[72:75], v[200:203], v[172:175], v[72:75]\n\t"
    "ds_read_b64_tr_b16 v[234:235], v251 offset:256\n\t"
    "v_mfma_f32_16x16x32_bf16 v[76:79], v[204:207], v[172:175], v[76:79]\n\t"
    "ds_read_b128 v[172:175], v241 offset:6144\n\t"
    "s_waitcnt lgkmcnt(15)\n\t"
    "v_mfma_f32_16x16x32_bf16 v[80:83], v[192:195], v[176:179], v[80:83]\n\t"
    "ds_read_b64_tr_b16 v[236:237], v251 offset:32\n\t"
    "v_mfma_f32_16x16x32_bf16 v[84:87], v[196:199], v[176:179], v[84:87]\n\t"
    "ds_read_b64_tr_b16 v[238:239], v251 offset:288\n\t"
    "v_mfma_f32_16x16x32_bf16 v[88:91], v[200:203], v[176:179], v[88:91]\n\t"
    "v_mfma_f32_16x16x32_bf16 v[92:95], v[204:207], v[176:179], v[92:95]\n\t"
    "ds_read_b128 v[176:179], v241 offset:8192\n\t"
    "s_waitcnt lgkmcnt(15)\n\t"
    "v_mfma_f32_16x16x32_bf16 v[96:99], v[192:195], v[180:183], v[96:99]\n\t"
    "v_mfma_f32_16x16x32_bf16 v[100:103], v[196:199], v[180:183], v[100:103]\n\t"
    "v_mfma_f32_16x16x32_bf16 v[104:107], v[200:203], v[180:183], v[104:107]\n\t"
    "v_mfma_f32_16x16x32_bf16 v[108:111], v[204:207], v[180:183], v[108:111]\n\t"
    "ds_read_b128 v[180:183], v241 offset:10240\n\t"
    "s_waitcnt lgkmcnt(15)\n\t"
    "v_mfma_f32_16x16x32_bf16 v[112:115], v[192:195], v[184:187], v[112:115]\n\t"
    "v_mfma_f32_16x16x32_bf16 v[116:119], v[196:199], v[184:187], v[116:119]\n\t"
    "v_mfma_f32_16x16x32_bf16 v[120:123], v[200:203], v[184:187], v[120:123]\n\t"
    "v_mfma_f32_16x16x32_bf16 v[124:127], v[204:207], v[184:187], v[124:127]\n\t"
    "ds_read_b128 v[184:187], v241 offset:12288\n\t"
    "s_waitcnt lgkmcnt(15)\n\t"
    "v_mfma_f32_16x16x32_bf16 v[128:131], v[192:195], v[188:191], v[128:131]\n\t"
    "v_mfma_f32_16x16x32_bf16 v[132:135], v[196:199], v[188:191], v[132:135]\n\t"
    "v_mfma_f32_16x16x32_bf16 v[136:139], v[200:203], v[188:191], v[136:139]\n\t"
    "v_mfma_f32_16x16x32_bf16 v[140:143], v[204:207], v[188:191], v[140:143]\n\t"
    "ds_read_b128 v[188:191], v241 offset:14336\n\t"
    "s_waitcnt lgkmcnt(13)\n\t"
    "v_mfma_f32_16x16x32_bf16 v[16:19], v[208:211], v[160:163], v[16:19]\n\t"
    "s_waitcnt lgkmcnt(4)\n\t"
    "v_mfma_f32_16x16x32_bf16 v[144:147], v[232:235], v[224:227], v[144:147]\n\t"
    "v_mfma_f32_16x16x32_bf16 v[20:23], v[212:215], v[160:163], v[20:23]\n\t"
    "v_mfma_f32_16x16x32_bf16 v[148:151], v[236:239], v[224:227], v[148:151]\n\t"
    "v_mfma_f32_16x16x32_bf16 v[24:27], v[216:219], v[160:163], v[24:27]\n\t"
    "v_mfma_f32_16x16x32_bf16 v[152:155], v[232:235], v[228:231], v[152:155]\n\t"
    "v_mfma_f32_16x16x32_bf16 v[28:31], v[220:223], v[160:163], v[28:31]\n\t"
    "v_mfma_f32_16x16x32_bf16 v[156:159], v[236:239], v[228:231], v[156:159]\n\t"
    "v_mfma_f32_16x16x32_bf16 v[32:35], v[208:211], v[164:167], v[32:35]\n\t"
    "v_mfma_f32_16x16x32_bf16 v[36:39], v[212:215], v[164:167], v[36:39]\n\t"
    "v_mfma_f32_16x16x32_bf16 v[40:43], v[216:219], v[164:167], v[40:43]\n\t"
    "v_mfma_f32_16x16x32_bf16 v[44:47], v[220:223], v[164:167], v[44:47]\n\t"
    "v_mfma_f32_16x16x32_bf16 v[48:51], v[208:211], v[168:171], v[48:51]\n\t"
    "v_mfma_f32_16x16x32_bf16 v[52:55], v[212:215], v[168:171], v[52:55]\n\t"
    "v_mfma_f32_16x16x32_bf16 v[56:59], v[216:219], v[168:171], v[56:59]\n\t"
    "v_mfma_f32_16x16x32_bf16 v[60:63], v[220:223], v[168:171], v[60:63]\n\t"
    "s_waitcnt lgkmcnt(0)\n\t"
    "s_barrier\n\t"
    "s_add_u32 s61, s61, 1\n\t"
    "s_cmp_eq_u32 s61, 3\n\t"
    "s_cselect_b32 s62, s64, s63\n\t"
    "s_cselect_b32 s65, s67, s66\n\t"
    "s_cselect_b32 s61, 0, s61\n\t"
    "v_add_u32 v240, s62, v240\n\t"
    "v_add_u32 v241, s62, v241\n\t"
    "v_add_u32 v242, s62, v242\n\t"
    "v_add_u32 v243, s62, v243\n\t"
    "v_add_u32 v244, s62, v244\n\t"
    "v_add_u32 v245, s62, v245\n\t"
    "v_add_u32 v246, s62, v246\n\t"
    "v_add_u32 v247, s62, v247\n\t"
    "v_add_u32 v248, s62, v248\n\t"
    "v_add_u32 v249, s62, v249\n\t"
    "v_add_u32 v250, s62, v250\n\t"
    "v_add_u32 v251, s65, v251\n\t"
    "ds_read_b64_tr_b16 v[192:193], v242 offset:32768\n\t"
    "v_mfma_f32_16x16x32_bf16 v[64:67], v[208:211], v[172:175], v[64:67]\n\t"
    "ds_read_b64_tr_b16 v[194:195], v243 offset:32768\n\t"
    "v_mfma_f32_16x16x32_bf16 v[68:71], v[212:215], v[172:175], v[68:71]\n\t"
    "ds_read_b64_tr_b16 v[196:197], v244 offset:32768\n\t"
    "v_mfma_f32_16x16x32_bf16 v[72:75], v[216:219], v[172:175], v[72:75]\n\t"
    "ds_read_b64_tr_b16 v[198:199], v245 offset:32768\n\t"
    "v_mfma_f32_16x16x32_bf16 v[76:79], v[220:223], v[172:175], v[76:79]\n\t"
    "ds_read_b64_tr_b16 v[200:201], v246 offset:32768\n\t"
    "v_mfma_f32_16x16x32_bf16 v[80:83], v[208:211], v[176:179], v[80:83]\n\t"
    "ds_read_b64_tr_b16 v[202:203], v247 offset:32768\n\t"
    "v_mfma_f32_16x16x32_bf16 v[84:87], v[212:215], v[176:179], v[84:87]\n\t"
    "ds_read_b64_tr_b16 v[204:205], v248 offset:32768\n\t"
    "v_mfma_f32_16x16x32_bf16 v[88:91], v[216:219], v[176:179], v[88:91]\n\t"
    "ds_read_b64_tr_b16 v[206:207], v249 offset:32768\n\t"
    "v_mfma_f32_16x16x32_bf16 v[92:95], v[220:223], v[176:179], v[92:95]\n\t"
    "ds_read_b128 v[160:163], v240 offset:0\n\t"
    "v_mfma_f32_16x16x32_bf16 v[96:99], v[208:211], v[180:183], v[96:99]\n\t"
    "ds_read_b128 v[164:167], v240 offset:2048\n\t"
    "v_mfma_f32_16x16x32_bf16 v[100:103], v[212:215], v[180:183], v[100:103]\n\t"
    "ds_read_b128 v[168:171], v240 offset:4096\n\t"
    "v_mfma_f32_16x16x32_bf16 v[104:107], v[216:219], v[180:183], v[104:107]\n\t"
    "ds_read_b128 v[172:175], v240 offset:6144\n\t"
    "v_mfma_f32_16x16x32_bf16 v[108:111], v[220:223], v[180:183], v[108:111]\n\t"
    "ds_read_b128 v[176:179], v240 offset:8192\n\t"
    "v_mfma_f32_16x16x32_bf16 v[112:115], v[208:211], v[184:187], v[112:115]\n\t"
    "ds_read_b128 v[180:183], v240 offset:10240\n\t"
    "v_mfma_f32_16x16x32_bf16 v[116:119], v[212:215], v[184:187], v[116:119]\n\t"
    "v_mfma_f32_16x16x32_bf16 v[120:123], v[216:219], v[184:187], v[120:123]\n\t"
    "v_mfma_f32_16x16x32_bf16 v[124:127], v[220:223], v[184:187], v[124:127]\n\t"
    "ds_read_b128 v[184:187], v240 offset:12288\n\t"
    "v_mfma_f32_16x16x32_bf16 v[128:131], v[208:211], v[188:191], v[128:131]\n\t"
    "v_mfma_f32_16x16x32_bf16 v[132:135], v[212:215], v[188:191], v[132:135]\n\t"
    "v_mfma_f32_16x16x32_bf16 v[136:139], v[216:219], v[188:191], v[136:139]\n\t"
    "v_mfma_f32_16x16x32_bf16 v[140:143], v[220:223], v[188:191], v[140:143]\n\t"
    "ds_read_b128 v[188:191], v240 offset:14336\n\t"
    "s_sub_u32 s60, s60, 1\n\t"
    "s_cmp_lg_u32 s60, 0\n\t"
    "s_cbranch_scc1 LOOP_%=\n\t"
    "s_waitcnt lgkmcnt(0)\n\t"
    "s_nop 7\n\t"
    "s_nop 3\n\t"
    : "+{v[16:31]}"(acc[0]), "+{v[32:47]}"(acc[1]), "+{v[48:63]}"(acc[2]), "+{v[64:79]}"(acc[3]), "+{v[80:95]}"(acc[4]), "+{v[96:111]}"(acc[5]), "+{v[112:127]}"(acc[6]), "+{v[128:143]}"(acc[7]), "+{v[144:159]}"(acc[8]), "+{v240}"(xa[0]), "+{v241}"(xa[1]), "+{v242}"(wa[0]), "+{v243}"(wa[1]), "+{v244}"(wa[2]), "+{v245}"(wa[3]), "+{v246}"(wa[4]), "+{v247}"(wa[5]), "+{v248}"(wa[6]), "+{v249}"(wa[7]), "+{v250}"(ea[0]), "+{v251}"(ea[1])
    :
    : "memory", "scc", "v160", "v161", "v162", "v163", "v164", "v165", "v166", "v167", "v168", "v169", "v170", "v171", "v172", "v173", "v174", "v175", "v176", "v177", "v178", "v179", "v180", "v181", "v182", "v183", "v184", "v185", "v186", "v187", "v188", "v189", "v190", "v191", "v192", "v193", "v194", "v195", "v196", "v197", "v198", "v199", "v200", "v201", "v202", "v203", "v204", "v205", "v206", "v207", "v208", "v209", "v210", "v211", "v212", "v213", "v214", "v215", "v216", "v217", "v218", "v219", "v220", "v221", "v222", "v223", "v224", "v225", "v226", "v227", "v228", "v229", "v230", "v231", "v232", "v233", "v234", "v235", "v236", "v237", "v238", "v239", "s60", "s61", "s62", "s63", "s64", "s65", "s66", "s67");
}

DI void proj_core_wx_v(f32x16* acc, unsigned* xa, unsigned* wa, unsigned* ea) {
  asm volatile(
    "s_mov_b32 s66, 4096\n\t"
    "s_mov_b32 s67, 4294959104\n\t"
    "s_mov_b32 s60, 16\n\t"
    "s_mov_b32 s61, 0\n\t"
    "s_mov_b32 s63, 49152\n\t"
    "s_mov_b32 s64, 4294868992\n\t"
    "s_barrier\n\t"
    "ds_read_b64_tr_b16 v[192:193], v242 offset:32768\n\t"
    "ds_read_b64_tr_b16 v[194:195], v243 offset:32768\n\t"
    "ds_read_b64_tr_b16 v[196:197], v244 offset:32768\n\t"
    "ds_read_b64_tr_b16 v[198:199], v245 offset:32768\n\t"
    "ds_read_b64_tr_b16 v[200:201], v246 offset:32768\n\t"
    "ds_read_b64_tr_b16 v[202:203], v247 offset:32768\n\t"
    "ds_read_b64_tr_b16 v[204:205], v248 offset:32768\n\t"
    "ds_read_b64_tr_b16 v[206:207], v249 offset:32768\n\t"
    "ds_read_b128 v[160:163], v240 offset:0\n\t"
    "ds_read_b128 v[164:167], v240 offset:2048\n\t"
    "ds_read_b128 v[168:171], v240 offset:4096\n\t"
    "ds_read_b128 v[172:175], v240 offset:6144\n\t"
    "ds_read_b128 v[176:179], v240 offset:8192\n\t"
    "ds_read_b128 v[180:183], v240 offset:10240\n\t"
    "ds_read_b128 v[184:187], v240 offset:12288\n\t"
    "ds_read_b128 v[188:191], v240 offset:14336\n\t"
    "LOOP_%=:\n\t"
    "s_waitcnt lgkmcnt(7)\n\t"
    "v_mfma_f32_16x16x32_bf16 v[16:19], v[192:195], v[160:163], v[16:19]\n\t"
    "ds_read_b64_tr_b16 v[208:209], v242 offset:40960\n\t"
    "v_mfma_f32_16x16x32_bf16 v[20:23], v[196:199], v[160:163], v[20:23]\n\t"
    "ds_read_b64_tr_b16 v[210:211], v243 offset:40960\n\t"
    "v_mfma_f32_16x16x32_bf16 v[24:27], v[200:203], v[160:163], v[24:27]\n\t"
    "ds_read_b64_tr_b16 v[212:213], v244 offset:40960\n\t"
    "v_mfma_f32_16x16x32_bf16 v[28:31], v[204:207], v[160:163], v[28:31]\n\t"
    "ds_read_b64_tr_b16 v[214:215], v245 offset:40960\n\t"
    "s_waitcnt lgkmcnt(10)\n\t"
    "v_mfma_f32_16x16x32_bf16 v[32:35], v[192:195], v[164:167], v[32:35]\n\t"
    "ds_read_b64_tr_b16 v[216:217], v246 offset:40960\n\t"
    "v_mfma_f32_16x16x32_bf16 v[36:39], v[196:199], v[164:167], v[36:39]\n\t"
    "ds_read_b64_tr_b16 v[218:219], v247 offset:40960\n\t"
    "v_mfma_f32_16x16x32_bf16 v[40:43], v[200:203], v[164:167], v[40:43]\n\t"
    "ds_read_b64_tr_b16 v[220:221], v248 offset:40960\n\t"
    "v_mfma_f32_16x16x32_bf16 v[44:47], v[204:207], v[164:167], v[44:47]\n\t"
    "ds_read_b64_tr_b16 v[222:223], v249 offset:40960\n\t"
    "s_waitcnt lgkmcnt(13)\n\t"
    "v_mfma_f32_16x16x32_bf16 v[48:51], v[192:195], v[168:171], v[48:51]\n\t"
    "ds_read_b128 v[160:163], v241 offset:0\n\t"
    "v_mfma_f32_16x16x32_bf16 v[52:55], v[196:199], v[168:171], v[52:55]\n\t"
    "ds_read_b128 v[164:167], v241 offset:2048\n\t"
    "v_mfma_f32_16x16x32_bf16 v[56:59], v[200:203], v[168:171], v[56:59]\n\t"
    "ds_read_b128 v[224:227], v250 offset:0\n\t"
    "v_mfma_f32_16x16x32_bf16 v[60:63], v[204:207], v[168:171], v[60:63]\n\t"
    "ds_read_b128 v[168:171], v241 offset:4096\n\t"
    "s_waitcnt lgkmcnt(15)\n\t"
    "v_mfma_f32_16x16x32_bf16 v[64:67], v[192:195], v[172:175], v[64:67]\n\t"
    "ds_read_b128 v[228:231], v250 offset:2048\n\t"
    "v_mfma_f32_16x16x32_bf16 v[68:71], v[196:199], v[172:175], v[68:71]\n\t"
    "ds_read_b64_tr_b16 v[232:233], v251 offset:0\n\t"
    "v_mfma_f32_16x16x32_bf16 v[72:75], v[200:203], v[172:175], v[72:75]\n\t"
    "ds_read_b64_tr_b16 v[234:235], v251 offset:256\n\t"
    "v_mfma_f32_16x16x32_bf16 v[76:79], v[204:207], v[172:175], v[76:79]\n\t"
    "ds_read_b128 v[172:175], v241 offset:6144\n\t"
    "s_waitcnt lgkmcnt(15)\n\t"
    "v_mfma_f32_16x16x32_bf16 v[80:83], v[192:195], v[176:179], v[80:83]\n\t"
    "ds_read_b64_tr_b16 v[236:237], v251 offset:32\n\t"
    "v_mfma_f32_16x16x32_bf16 v[84:87], v[196:199], v[176:179], v[84:87]\n\t"
    "ds_read_b64_tr_b16 v[238:239], v251 offset:288\n\t"
    "v_mfma_f32_16x16x32_bf16 v[88:91], v[200:203], v[176:179], v[88:91]\n\t"
    "v_mfma_f32_16x16x32_bf16 v[92:95], v[204:207], v[176:179], v[92:95]\n\t"
    "ds_read_b128 v[176:179], v241 offset:8192\n\t"
    "s_waitcnt lgkmcnt(15)\n\t"
    "v_mfma_f32_16x16x32_bf16 v[96:99], v[192:195], v[180:183], v[96:99]\n\t"
    "v_mfma_f32_16x16x32_bf16 v[100:103], v[196:199], v[180:183], v[100:103]\n\t"
    "v_mfma_f32_16x16x32_bf16 v[104:107], v[200:203], v[180:183], v[104:107]\n\t"
    "v_mfma_f32_16x16x32_bf16 v[108:111], v[204:207], v[180:183], v[108:111]\n\t"
    "ds_read_b128 v[180:183], v241 offset:10240\n\t"
    "s_waitcnt lgkmcnt(15)\n\t"
    "v_mfma_f32_16x16x32_bf16 v[112:115], v[192:195], v[184:187], v[112:115]\n\t"
    "v_mfma_f32_16x16x32_bf16 v[116:119], v[196:199], v[184:187], v[116:119]\n\t"
    "v_mfma_f32_16x16x32_bf16 v[120:123], v[200:203], v[184:187], v[120:123]\n\t"
    "v_mfma_f32_16x16x32_bf16 v[124:127], v[204:207], v[184:187], v[124:127]\n\t"
    "ds_read_b128 v[184:187], v241 offset:12288\n\t"
    "s_waitcnt lgkmcnt(15)\n\t"
    "v_mfma_f32_16x16x32_bf16 v[128:131], v[192:195], v[188:191], v[128:131]\n\t"
    "v_mfma_f32_16x16x32_bf16 v[132:135], v[196:199], v[188:191], v[132:135]\n\t"
    "v_mfma_f32_16x16x32_bf16 v[136:139], v[200:203], v[188:191], v[136:139]\n\t"
    "v_mfma_f32_16x16x32_bf16 v[140:143], v[204:207], v[188:191], v[140:143]\n\t"
    "ds_read_b128 v[188:191], v241 offset:14336\n\t"
    "s_waitcnt lgkmcnt(13)\n\t"
    "v_mfma_f32_16x16x32_bf16 v[16:19], v[208:211], v[160:163], v[16:19]\n\t"
    "s_waitcnt lgkmcnt(4)\n\t"
    "v_mfma_f32_16x16x32_bf16 v[144:147], v[224:227], v[232:235], v[144:147]\n\t"
    "v_mfma_f32_16x16x32_bf16 v[20:23], v[212:215], v[160:163], v[20:23]\n\t"
    "v_mfma_f32_16x16x32_bf16 v[148:151], v[224:227], v[236:239], v[148:151]\n\t"
    "v_mfma_f32_16x16x32_bf16 v[24:27], v[216:219], v[160:163], v[24:27]\n\t"
    "v_mfma_f32_16x16x32_bf16 v[152:155], v[228:231], v[232:235], v[152:155]\n\t"
    "v_mfma_f32_16x16x32_bf16 v[28:31], v[220:223], v[160:163], v[28:31]\n\t"
    "v_mfma_f32_16x16x32_bf16 v[156:159], v[228:231], v[236:239], v[156:159]\n\t"
    "v_mfma_f32_16x16x32_bf16 v[32:35], v[208:211], v[164:167], v[32:35]\n\t"
    "v_mfma_f32_16x16x32_bf16 v[36:39], v[212:215], v[164:167], v[36:39]\n\t"
    "v_mfma_f32_16x16x32_bf16 v[40:43], v[216:219], v[164:167], v[40:43]\n\t"
    "v_mfma_f32_16x16x32_bf16 v[44:47], v[220:223], v[164:167], v[44:47]\n\t"
    "v_mfma_f32_16x16x32_bf16 v[48:51], v[208:211], v[168:171], v[48:51]\n\t"
    "v_mfma_f32_16x16x32_bf16 v[52:55], v[212:215], v[168:171], v[52:55]\n\t"
    "v_mfma_f32_16x16x32_bf16 v[56:59], v[216:219], v[168:171], v[56:59]\n\t"
    "v_mfma_f32_16x16x32_bf16 v[60:63], v[220:223], v[168:171], v[60:63]\n\t"
    "s_waitcnt lgkmcnt(0)\n\t"
    "s_barrier\n\t"
    "s_add_u32 s61, s61, 1\n\t"
    "s_cmp_eq_u32 s61, 3\n\t"
    "s_cselect_b32 s62, s64, s63\n\t"
    "s_cselect_b32 s65, s67, s66\n\t"
    "s_cselect_b32 s61, 0, s61\n\t"
    "v_add_u32 v240, s62, v240\n\t"
    "v_add_u32 v241, s62, v241\n\t"
    "v_add_u32 v242, s62, v242\n\t"
    "v_add_u32 v243, s62, v243\n\t"
    "v_add_u32 v244, s62, v244\n\t"
    "v_add_u32 v245, s62, v245\n\t"
    "v_add_u32 v246, s62, v246\n\t"
    "v_add_u32 v247, s62, v247\n\t"
    "v_add_u32 v248, s62, v248\n\t"
    "v_add_u32 v249, s62, v249\n\t"
    "v_add_u32 v250, s62, v250\n\t"
    "v_add_u32 v251, s65, v251\n\t"
    "ds_read_b64_tr_b16 v[192:193], v242 offset:32768\n\t"
    "v_mfma_f32_16x16x32_bf16 v[64:67], v[208:211], v[172:175], v[64:67]\n\t"
    "ds_read_b64_tr_b16 v[194:195], v243 offset:32768\n\t"
    "v_mfma_f32_16x16x32_bf16 v[68:71], v[212:215], v[172:175], v[68:71]\n\t"
    "ds_read_b64_tr_b16 v[196:197], v244 offset:32768\n\t"
    "v_mfma_f32_16x16x32_bf16 v[72:75], v[216:219], v[172:175], v[72:75]\n\t"
    "ds_read_b64_tr_b16 v[198:199], v245 offset:32768\n\t"
    "v_mfma_f32_16x16x32_bf16 v[76:79], v[220:223], v[172:175], v[76:79]\n\t"
    "ds_read_b64_tr_b16 v[200:201], v246 offset:32768\n\t"
    "v_mfma_f32_16x16x32_bf16 v[80:83], v[208:211], v[176:179], v[80:83]\n\t"
    "ds_read_b64_tr_b16 v[202:203], v247 offset:32768\n\t"
    "v_mfma_f32_16x16x32_bf16 v[84:87], v[212:215], v[176:179], v[84:87]\n\t"
    "ds_read_b64_tr_b16 v[204:205], v248 offset:32768\n\t"
    "v_mfma_f32_16x16x32_bf16 v[88:91], v[216:219], v[176:179], v[88:91]\n\t"
    "ds_read_b64_tr_b16 v[206:207], v249 offset:32768\n\t"
    "v_mfma_f32_16x16x32_bf16 v[92:95], v[220:223], v[176:179], v[92:95]\n\t"
    "ds_read_b128 v[160:163], v240 offset:0\n\t"
    "v_mfma_f32_16x16x32_bf16 v[96:99], v[208:211], v[180:183], v[96:99]\n\t"
    "ds_read_b128 v[164:167], v240 offset:2048\n\t"
    "v_mfma_f32_16x16x32_bf16 v[100:103], v[212:215], v[180:183], v[100:103]\n\t"
    "ds_read_b128 v[168:171], v240 offset:4096\n\t"
    "v_mfma_f32_16x16x32_bf16 v[104:107], v[216:219], v[180:183], v[104:107]\n\t"
    "ds_read_b128 v[172:175], v240 offset:6144\n\t"
    "v_mfma_f32_16x16x32_bf16 v[108:111], v[220:223], v[180:183], v[108:111]\n\t"
    "ds_read_b128 v[176:179], v240 offset:8192\n\t"
    "v_mfma_f32_16x16x32_bf16 v[112:115], v[208:211], v[184:187], v[112:115]\n\t"
    "ds_read_b128 v[180:183], v240 offset:10240\n\t"
    "v_mfma_f32_16x16x32_bf16 v[116:119], v[212:215], v[184:187], v[116:119]\n\t"
    "v_mfma_f32_16x16x32_bf16 v[120:123], v[216:219], v[184:187], v[120:123]\n\t"
    "v_mfma_f32_16x16x32_bf16 v[124:127], v[220:223], v[184:187], v[124:127]\n\t"
    "ds_read_b128 v[184:187], v240 offset:12288\n\t"
    "v_mfma_f32_16x16x32_bf16 v[128:131], v[208:211], v[188:191], v[128:131]\n\t"
    "v_mfma_f32_16x16x32_bf16 v[132:135], v[212:215], v[188:191], v[132:135]\n\t"
    "v_mfma_f32_16x16x32_bf16 v[136:139], v[216:219], v[188:191], v[136:139]\n\t"
    "v_mfma_f32_16x16x32_bf16 v[140:143], v[220:223], v[188:191], v[140:143]\n\t"
    "ds_read_b128 v[188:191], v240 offset:14336\n\t"
    "s_sub_u32 s60, s60, 1\n\t"
    "s_cmp_lg_u32 s60, 0\n\t"
    "s_cbranch_scc1 LOOP_%=\n\t"
    "s_waitcnt lgkmcnt(0)\n\t"
    "s_nop 7\n\t"
    "s_nop 3\n\t"
    : "+{v[16:31]}"(acc[0]), "+{v[32:47]}"(acc[1]), "+{v[48:63]}"(acc[2]), "+{v[64:79]}"(acc[3]), "+{v[80:95]}"(acc[4]), "+{v[96:111]}"(acc[5]), "+{v[112:127]}"(acc[6]), "+{v[128:143]}"(acc[7]), "+{v[144:159]}"(acc[8]), "+{v240}"(xa[0]), "+{v241}"(xa[1]), "+{v242}"(wa[0]), "+{v243}"(wa[1]), "+{v244}"(wa[2]), "+{v245}"(wa[3]), "+{v246}"(wa[4]), "+{v247}"(wa[5]), "+{v248}"(wa[6]), "+{v249}"(wa[7]), "+{v250}"(ea[0]), "+{v251}"(ea[1])
    :
    : "memory", "scc", "v160", "v161", "v162", "v163", "v164", "v165", "v166", "v167", "v168", "v169", "v170", "v171", "v172", "v173", "v174", "v175", "v176", "v177", "v178", "v179", "v180", "v181", "v182", "v183", "v184", "v185", "v186", "v187", "v188", "v189", "v190", "v191", "v192", "v193", "v194", "v195", "v196", "v197", "v198", "v199", "v200", "v201", "v202", "v203", "v204", "v205", "v206", "v207", "v208", "v209", "v210", "v211", "v212", "v213", "v214", "v215", "v216", "v217", "v218", "v219", "v220", "v221", "v222", "v223", "v224", "v225", "v226", "v227", "v228", "v229", "v230", "v231", "v232", "v233", "v234", "v235", "v236", "v237", "v238", "v239", "s60", "s61", "s62", "s63", "s64", "s65", "s66", "s67");
}

DI void out_core(f32x16* acc, unsigned* xa, unsigned* wa) {
  asm volatile(
    "s_mov_b32 s60, 16\n\t"
    "s_mov_b32 s61, 0\n\t"
    "s_mov_b32 s63, 32768\n\t"
    "s_mov_b32 s64, 4294868992\n\t"
    "s_barrier\n\t"
    "ds_read_b64_tr_b16 v[144:145], v177 offset:16384\n\t"
    "ds_read_b64_tr_b16 v[146:147], v178 offset:16384\n\t"
    "ds_read_b64_tr_b16 v[148:149], v179 offset:16384\n\t"
    "ds_read_b64_tr_b16 v[150:151], v180 offset:16384\n\t"
    "ds_read_b64_tr_b16 v[152:153], v181 offset:16384\n\t"
    "ds_read_b64_tr_b16 v[154:155], v182 offset:16384\n\t"
    "ds_read_b64_tr_b16 v[156:157], v183 offset:16384\n\t"
    "ds_read_b64_tr_b16 v[158:159], v184 offset:16384\n\t"
    "ds_read_b128 v[128:131], v176 offset:0\n\t"
    "ds_read_b128 v[132:135], v176 offset:256\n\t"
    "ds_read_b128 v[136:139], v176 offset:4096\n\t"
    "ds_read_b128 v[140:143], v176 offset:4352\n\t"
    "LOOP_%=:\n\t"
    "s_waitcnt lgkmcnt(3)\n\t"
    "v_mfma_f32_16x16x32_bf16 v[64:67], v[144:147], v[128:131], v[64:67]\n\t"
    "ds_read_b64_tr_b16 v[160:161], v177 offset:24576\n\t"
    "v_mfma_f32_16x16x32_bf16 v[68:71], v[148:151], v[128:131], v[68:71]\n\t"
    "ds_read_b64_tr_b16 v[162:163], v178 offset:24576\n\t"
    "v_mfma_f32_16x16x32_bf16 v[72:75], v[152:155], v[128:131], v[72:75]\n\t"
    "ds_read_b64_tr_b16 v[164:165], v179 offset:24576\n\t"
    "v_mfma_f32_16x16x32_bf16 v[76:79], v[156:159], v[128:131], v[76:79]\n\t"
    "ds_read_b64_tr_b16 v[166:167], v180 offset:24576\n\t"
    "s_waitcnt lgkmcnt(6)\n\t"
    "v_mfma_f32_16x16x32_bf16 v[80:83], v[144:147], v[132:135], v[80:83]\n\t"
    "ds_read_b64_tr_b16 v[168:169], v181 offset:24576\n\t"
    "v_mfma_f32_16x16x32_bf16 v[84:87], v[148:151], v[132:135], v[84:87]\n\t"
    "ds_read_b64_tr_b16 v[170:171], v182 offset:24576\n\t"
    "v_mfma_f32_16x16x32_bf16 v[88:91], v[152:155], v[132:135], v[88:91]\n\t"
    "ds_read_b64_tr_b16 v[172:173], v183 offset:24576\n\t"
    "v_mfma_f32_16x16x32_bf16 v[92:95], v[156:159], v[132:135], v[92:95]\n\t"
    "ds_read_b64_tr_b16 v[174:175], v184 offset:24576\n\t"
    "s_waitcnt lgkmcnt(9)\n\t"
    "v_mfma_f32_16x16x32_bf16 v[96:99], v[144:147], v[136:139], v[96:99]\n\t"
    "ds_read_b128 v[128:131], v176 offset:2048\n\t"
    "v_mfma_f32_16x16x32_bf16 v[100:103], v[148:151], v[136:139], v[100:103]\n\t"
    "ds_read_b128 v[132:135], v176 offset:2304\n\t"
    "v_mfma_f32_16x16x32_bf16 v[104:107], v[152:155], v[136:139], v[104:107]\n\t"
    "v_mfma_f32_16x16x32_bf16 v[108:111], v[156:159], v[136:139], v[108:111]\n\t"
    "ds_read_b128 v[136:139], v176 offset:6144\n\t"
    "s_waitcnt lgkmcnt(11)\n\t"
    "v_mfma_f32_16x16x32_bf16 v[112:115], v[144:147], v[140:143], v[112:115]\n\t"
    "v_mfma_f32_16x16x32_bf16 v[116:119], v[148:151], v[140:143], v[116:119]\n\t"
    "v_mfma_f32_16x16x32_bf16 v[120:123], v[152:155], v[140:143], v[120:123]\n\t"
    "v_mfma_f32_16x16x32_bf16 v[124:127], v[156:159], v[140:143], v[124:127]\n\t"
    "ds_read_b128 v[140:143], v176 offset:6400\n\t"
    "s_waitcnt lgkmcnt(3)\n\t"
    "v_mfma_f32_16x16x32_bf16 v[64:67], v[160:163], v[128:131], v[64:67]\n\t"
    "v_mfma_f32_16x16x32_bf16 v[68:71], v[164:167], v[128:131], v[68:71]\n\t"
    "v_mfma_f32_16x16x32_bf16 v[72:75], v[168:171], v[128:131], v[72:75]\n\t"
    "v_mfma_f32_16x16x32_bf16 v[76:79], v[172:175], v[128:131], v[76:79]\n\t"
    "s_waitcnt lgkmcnt(2)\n\t"
    "v_mfma_f32_16x16x32_bf16 v[80:83], v[160:163], v[132:135], v[80:83]\n\t"
    "v_mfma_f32_16x16x32_bf16 v[84:87], v[164:167], v[132:135], v[84:87]\n\t"
    "s_waitcnt lgkmcnt(0)\n\t"
    "s_barrier\n\t"
    "s_add_u32 s61, s61, 1\n\t"
    "s_cmp_eq_u32 s61, 4\n\t"
    "s_cselect_b32 s62, s64, s63\n\t"
    "s_cselect_b32 s61, 0, s61\n\t"
    "v_add_u32 v176, s62, v176\n\t"
    "v_add_u32 v177, s62, v177\n\t"
    "v_add_u32 v178, s62, v178\n\t"
    "v_add_u32 v179, s62, v179\n\t"
    "v_add_u32 v180, s62, v180\n\t"
    "v_add_u32 v181, s62, v181\n\t"
    "v_add_u32 v182, s62, v182\n\t"
    "v_add_u32 v183, s62, v183\n\t"
    "v_add_u32 v184, s62, v184\n\t"
    "ds_read_b64_tr_b16 v[144:145], v177 offset:16384\n\t"
    "ds_read_b64_tr_b16 v[146:147], v178 offset:16384\n\t"
    "v_mfma_f32_16x16x32_bf16 v[88:91], v[168:171], v[132:135], v[88:91]\n\t"
    "ds_read_b64_tr_b16 v[148:149], v179 offset:16384\n\t"
    "ds_read_b64_tr_b16 v[150:151], v180 offset:16384\n\t"
    "v_mfma_f32_16x16x32_bf16 v[92:95], v[172:175], v[132:135], v[92:95]\n\t"
    "ds_read_b64_tr_b16 v[152:153], v181 offset:16384\n\t"
    "v_mfma_f32_16x16x32_bf16 v[96:99], v[160:163], v[136:139], v[96:99]\n\t"
    "ds_read_b64_tr_b16 v[154:155], v182 offset:16384\n\t"
    "v_mfma_f32_16x16x32_bf16 v[100:103], v[164:167], v[136:139], v[100:103]\n\t"
    "ds_read_b64_tr_b16 v[156:157], v183 offset:16384\n\t"
    "v_mfma_f32_16x16x32_bf16 v[104:107], v[168:171], v[136:139], v[104:107]\n\t"
    "ds_read_b64_tr_b16 v[158:159], v184 offset:16384\n\t"
    "v_mfma_f32_16x16x32_bf16 v[108:111], v[172:175], v[136:139], v[108:111]\n\t"
    "ds_read_b128 v[128:131], v176 offset:0\n\t"
    "v_mfma_f32_16x16x32_bf16 v[112:115], v[160:163], v[140:143], v[112:115]\n\t"
    "ds_read_b128 v[132:135], v176 offset:256\n\t"
    "v_mfma_f32_16x16x32_bf16 v[116:119], v[164:167], v[140:143], v[116:119]\n\t"
    "ds_read_b128 v[136:139], v176 offset:4096\n\t"
    "v_mfma_f32_16x16x32_bf16 v[120:123], v[168:171], v[140:143], v[120:123]\n\t"
    "v_mfma_f32_16x16x32_bf16 v[124:127], v[172:175], v[140:143], v[124:127]\n\t"
    "ds_read_b128 v[140:143], v176 offset:4352\n\t"
    "s_sub_u32 s60, s60, 1\n\t"
    "s_cmp_lg_u32 s60, 0\n\t"
    "s_cbranch_scc1 LOOP_%=\n\t"
    "s_waitcnt lgkmcnt(0)\n\t"
    "s_nop 7\n\t"
    "s_nop 3\n\t"
    : "+{v[64:79]}"(acc[0]), "+{v[80:95]}"(acc[1]), "+{v[96:111]}"(acc[2]), "+{v[112:127]}"(acc[3]), "+{v176}"(xa[0]), "+{v177}"(wa[0]), "+{v178}"(wa[1]), "+{v179}"(wa[2]), "+{v180}"(wa[3]), "+{v181}"(wa[4]), "+{v182}"(wa[5]), "+{v183}"(wa[6]), "+{v184}"(wa[7])
    :
    : "memory", "scc", "v128", "v129", "v130", "v131", "v132", "v133", "v134", "v135", "v136", "v137", "v138", "v139", "v140", "v141", "v142", "v143", "v144", "v145", "v146", "v147", "v148", "v149", "v150", "v151", "v152", "v153", "v154", "v155", "v156", "v157", "v158", "v159", "v160", "v161", "v162", "v163", "v164", "v165", "v166", "v167", "v168", "v169", "v170", "v171", "v172", "v173", "v174", "v175", "s60", "s61", "s62", "s63", "s64", "s65", "s66", "s67");
}


constexpr int P_XB = 32768, P_WB = 16384, P_SLOT = P_XB + P_WB, P_KV = 3 * P_SLOT, P_KVB = 4096, P_LDS = P_KV + 3 * P_KVB;

DI void proj_loader(const char* __restrict__ X, const char* __restrict__ W, unsigned ldw, const char* __restrict__ Wx, bool extra,
                    int mblk, int rot, char* lds, int lane, int lw) {
  const unsigned lds0 = (unsigned)(uintptr_t)lds;
  const unsigned xrow = 64 * lw + (lane >> 3);
  const unsigned xo_e = xrow * 2048u + (((lane & 7) ^ (lane >> 4)) << 4);
  const unsigned xo_o = (xrow + 8) * 2048u + (((lane & 7) ^ (4 + (lane >> 4))) << 4);
  const char* xbase = X + (size_t)mblk * 256 * 2048;
  unsigned wo[4];
#pragma unroll
  for (int i = 0; i < 4; ++i) wo[i] = (16 * lw + 4 * i + (lane >> 4)) * ldw + (((lane & 15) ^ (((lane >> 4) << 2) | i)) << 4);
  const unsigned kvo = (16 * lw + (lane >> 2)) * 64u + (lane & 3) * 16u;
  auto issue = [&](int st, int slot) {
    const int ks = (st + rot) & 15;
    const unsigned sl = lds0 + slot * P_SLOT;
    const char* xb = xbase + ks * 128;
    const char* wb = W + (size_t)ks * 64 * ldw;
#pragma unroll
    for (int i = 0; i < 4; ++i) { glds16(xb + i * 32768, xo_e, sl + lw * 8192 + i * 2048); glds16(xb + i * 32768, xo_o, sl + lw * 8192 + i * 2048 + 1024); }
#pragma unroll
    for (int i = 0; i < 4; ++i) glds16(wb, wo[i], sl + P_XB + lw * 4096 + i * 1024);
    if (extra) glds16(Wx + ks * 4096, kvo, lds0 + P_KV + slot * P_KVB + lw * 1024);
  };
  issue(0, 0); issue(1, 1);
  if (extra) waitv<13>(); else waitv<12>();
  bar_l();
  issue(2, 2);
  int slot = 0;
#pragma unroll 1
  for (int j = 0; j < 16; ++j) {
    if (j >= 1 && j + 2 < 16) issue(j + 2, slot == 0 ? 2 : slot - 1);
    if (j + 2 < 16) { if (extra) waitv<13>(); else waitv<12>(); } else waitv<0>();
    bar_l();
    slot = slot == 2 ? 0 : slot + 1;
  }
  if (extra) bar_l();
}

DI u32x4 pair16(const f32x16& xa, int ca, const f32x16& xb, int cb, float sc) {
  const unsigned a0 = cvtpk(xa[4 * ca] * sc, xa[4 * ca + 1] * sc), a1 = cvtpk(xa[4 * ca + 2] * sc, xa[4 * ca + 3] * sc);
  const unsigned b0 = cvtpk(xb[4 * cb] * sc, xb[4 * cb + 1] * sc), b1 = cvtpk(xb[4 * cb + 2] * sc, xb[4 * cb + 3] * sc);
  const auto r0 = __builtin_amdgcn_permlane32_swap(a0, b0, false, false), r1 = __builtin_amdgcn_permlane32_swap(a1, b1, false, false);
  return (u32x4){r0[0], r1[0], r0[1], r1[1]};
}
template <int MODE, int EXTRA>
DI void proj_compute(char* dst, char* dst_extra, int mblk, int ntile, char* lds, int lane, int wid) {
  const unsigned lds0 = (unsigned)(uintptr_t)lds;
  const int wm = wid >> 1, wn = wid & 1, t16 = lane & 15, g = lane >> 4, q = t16 >> 2, p = lane & 3;
  unsigned xa[2], wa[8], ea[2];
#pragma unroll
  for (int s = 0; s < 2; ++s) xa[s] = lds0 + (128 * wm + t16) * 128 + (((4 * s + g) ^ (t16 >> 1)) << 4);
#pragma unroll
  for (int c = 0; c < 4; ++c)
#pragma unroll
    for (int t = 0; t < 2; ++t) wa[2 * c + t] = lds0 + (8 * g + 4 * t + q) * 256 + (((8 * wn + 2 * c + (p >> 1)) ^ ((q << 2) | ((2 * g + t) & 3))) << 4) + 8 * (p & 1);
  const int tbx = 2 * (ntile >> 1) + wn;
  ea[0] = lds0 + (32 * tbx + t16) * 128 + (((4 * wm + g) ^ (t16 >> 1)) << 4);
  ea[1] = lds0 + P_KV + (32 * wm + 8 * g + q) * 64 + p * 8;
  f32x16 acc[9] = {};
  if (EXTRA == 1)      proj_core_wx_k(acc, xa, wa, ea);
  else if (EXTRA == 2) proj_core_wx_v(acc, xa, wa, ea);
  else if (MODE != 2)  proj_core_wx(acc, xa, wa);
  else                 proj_core_xw(acc, xa, wa);
  if (EXTRA) {
    float* ex = (float*)(lds + wn * 4096);
    if (wm == 1) {
#pragma unroll
      for (int k = 0; k < 4; ++k) *(f32x4*)(ex + k * 256 + lane * 4) = (f32x4){acc[8][4 * k], acc[8][4 * k + 1], acc[8][4 * k + 2], acc[8][4 * k + 3]};
    }
    bar_l();
  }
  const float sc = (MODE == 0) ? QSCALE : 1.f;
  const unsigned lo = (32 * (g & 1) + 16 * (g >> 1) + t16) * 16;
#pragma unroll
  for (int hd = 0; hd < 2; ++hd)
#pragma unroll
    for (int tb = 0; tb < 4; ++tb) {
      const int head = 2 * wn + hd, t = mblk * 8 + 4 * wm + tb;
      char* o = dst + ((size_t)head * 64 + t) * (MODE == 0 ? 2048 : 4096) + lo;
#pragma unroll
      for (int s = 0; s < 2; ++s) {
        if (MODE != 2) st16(o + s * 1024, pair16(acc[2 * tb], 2 * hd + s, acc[2 * tb + 1], 2 * hd + s, sc));
        else           st16(o + s * 1024, pair16(acc[2 * tb + s], 2 * hd, acc[2 * tb + s], 2 * hd + 1, sc));
      }
    }
  if (EXTRA) {
    if (wm == 0) {
      const float* ex = (const float*)(lds + wn * 4096);
#pragma unroll
      for (int k = 0; k < 4; ++k) { const f32x4 v = *(const f32x4*)(ex + k * 256 + lane * 4); acc[8][4 * k] += v[0]; acc[8][4 * k + 1] += v[1]; acc[8][4 * k + 2] += v[2]; acc[8][4 * k + 3] += v[3]; }
      char* o = dst_extra + (size_t)(mblk * 8 + tbx) * 4096 + (EXTRA == 2 ? 2048 : 0) + lo;
#pragma unroll
      for (int s = 0; s < 2; ++s) {
        if (EXTRA == 1) st16(o + s * 1024, pair16(acc[8], s, acc[8], 2 + s, 1.f));
        else            st16(o + s * 1024, pair16(acc[8], 2 * s, acc[8], 2 * s + 1, 1.f));
      }
    }
  }
}

__global__ __launch_bounds__(512, 2) void k_proj(const char* __restrict__ xa, const char* __restrict__ wq, const char* __restrict__ wk, const char* __restrict__ wv,
                                                 const char* __restrict__ xb, const char* __restrict__ wqkv, char* __restrict__ ws) {
  extern __shared__ __attribute__((aligned(16))) char lds[];
  const int xcd = blockIdx.x & 7, idx = blockIdx.x >> 3, xm = xcd & 3, xn = xcd >> 2;
  const int lane = threadIdx.x & 63, wid = rfl(threadIdx.x >> 6);
  const bool isB = idx < 24;
  const int j = idx - 24;
  const int mblk = 2 * xm + ((isB ? idx : j) & 1), nt = isB ? 12 * xn + (idx >> 1) : 4 * xn + (j >> 1);
  if (__builtin_expect(wid >= 4, 1)) {
    proj_loader(isB ? xb : xa, (isB ? wqkv : wq) + nt * 256, isB ? 6144u : 2048u, (nt & 1) ? wv : wk, !isB, mblk, 2 * xcd, lds, lane, wid - 4);
    return;
  }
  if (isB) {
    if (nt < 8)       proj_compute<0, 0>(ws + WS_QB + (size_t)nt * 4 * 64 * 2048, nullptr, mblk, nt, lds, lane, wid);
    else if (nt < 16) proj_compute<1, 0>(ws + WS_KVB + (size_t)(nt - 8) * 4 * 64 * 4096, nullptr, mblk, nt, lds, lane, wid);
    else              proj_compute<2, 0>(ws + WS_KVB + (size_t)(nt - 16) * 4 * 64 * 4096 + 2048, nullptr, mblk, nt, lds, lane, wid);
  } else {
    if (nt & 1) proj_compute<0, 2>(ws + WS_QA + (size_t)nt * 4 * 64 * 2048, ws + WS_KVA, mblk, nt, lds, lane, wid);
    else        proj_compute<0, 1>(ws + WS_QA + (size_t)nt * 4 * 64 * 2048, ws + WS_KVA, mblk, nt, lds, lane, wid);
  }
}

DI void attn_exact(const char* __restrict__ qf, const char* __restrict__ kv, char* __restrict__ op, int lane) {
  const bf16x8 q0 = *(const bf16x8*)(qf + lane * 16), q1 = *(const bf16x8*)(qf + 1024 + lane * 16);
  float m = -INFINITY, l = 0.f; f32x16 o = {};
#pragma unroll 1
  for (int t = 0; t < 64; ++t) {
    const char* kp = kv + t * 4096 + lane * 16;
    const bf16x8 k0 = *(const bf16x8*)kp, k1 = *(const bf16x8*)(kp + 1024), v0 = *(const bf16x8*)(kp + 2048), v1 = *(const bf16x8*)(kp + 3072);
    f32x16 s = {}; s = MFMA32(k0, q0, s); s = MFMA32(k1, q1, s);
    float tm = s[0];
#pragma unroll
    for (int i = 1; i < 16; ++i) tm = fmaxf(tm, s[i]);
    tm = fmaxf(tm, __shfl_xor(tm, 32));
    const float mn = fmaxf(m, tm);
    const float alpha = __builtin_amdgcn_exp2f(m - mn);
    m = mn;
    float ps = 0.f;
#pragma unroll
    for (int i = 0; i < 16; ++i) { s[i] = __builtin_amdgcn_exp2f(s[i] - mn); ps += s[i]; }
    l = l * alpha + ps;
#pragma unroll
    for (int i = 0; i < 16; ++i) o[i] *= alpha;
    const u32x4 p0 = pack8(s, 0, 1.f), p1 = pack8(s, 1, 1.f);
    o = MFMA32(v0, __builtin_bit_cast(bf16x8, p0), o); o = MFMA32(v1, __builtin_bit_cast(bf16x8, p1), o);
  }
  l += __shfl_xor(l, 32);
  const float rl = 1.f / l;
  *(u32x4*)(op + lane * 16) = pack8(o, 0, rl); *(u32x4*)(op + 1024 + lane * 16) = pack8(o, 1, rl);
}

constexpr int AT_NST = 16, AT_R = 2, AT_SB = 16384;
DI void attn_fast_core(f32x16* o, f32x4* l, unsigned vaddr, const char* qptr, unsigned vl16, u32x4 ones, unsigned idx) {
  unsigned t_cnt, t_ring;
  asm volatile(
    "global_load_dwordx4 a[0:3], %12, %11 offset:-4096\n\t"
    "global_load_dwordx4 a[4:7], %12, %11 offset:-3072\n\t"
    "global_load_dwordx4 a[8:11], %12, %11 offset:-2048\n\t"
    "global_load_dwordx4 a[12:15], %12, %11 offset:-1024\n\t"
    "global_load_dwordx4 a[16:19], %12, %11 offset:0\n\t"
    "global_load_dwordx4 a[20:23], %12, %11 offset:1024\n\t"
    "global_load_dwordx4 a[24:27], %12, %11 offset:2048\n\t"
    "global_load_dwordx4 a[28:31], %12, %11 offset:3072\n\t"
    "v_mov_b32 v168, 0\n\t"
    "v_mov_b32 v169, 0\n\t"
    "v_mov_b32 v170, 0\n\t"
    "v_mov_b32 v171, 0\n\t"
    "v_mov_b32 v172, 0\n\t"
    "v_mov_b32 v173, 0\n\t"
    "v_mov_b32 v174, 0\n\t"
    "v_mov_b32 v175, 0\n\t"
    "v_accvgpr_write_b32 a56, 0\n\t"
    "v_accvgpr_write_b32 a57, 0\n\t"
    "v_accvgpr_write_b32 a58, 0\n\t"
    "v_accvgpr_write_b32 a59, 0\n\t"
    "v_accvgpr_write_b32 a60, 0\n\t"
    "v_accvgpr_write_b32 a61, 0\n\t"
    "v_accvgpr_write_b32 a62, 0\n\t"
    "v_accvgpr_write_b32 a63, 0\n\t"
    "s_waitcnt vmcnt(0)\n\t"
    "s_barrier\n\t"
    "ds_read_b128 a[32:35], %10 offset:0\n\t"
    "ds_read_b128 a[36:39], %10 offset:1024\n\t"
    "ds_read_b128 a[48:51], %10 offset:2048\n\t"
    "ds_read_b128 a[52:55], %10 offset:3072\n\t"
    "s_waitcnt lgkmcnt(0)\n\t"
    "v_mfma_f32_32x32x16_bf16 v[128:143], a[32:35], a[0:3], 0\n\t"
    "v_mfma_f32_32x32x16_bf16 v[128:143], a[36:39], a[4:7], v[128:143]\n\t"
    "s_nop 7\n\t"
    "s_nop 7\n\t"
    "s_mov_b32 %8, 8\n\t"
    "LOOP_%=:\n\t"
    "v_mfma_f32_32x32x16_bf16 v[144:159], a[32:35], a[8:11], 0\n\t"
    "ds_read_b128 a[40:43], %10 offset:4096\n\t"
    "v_exp_f32 v128, v128\n\t"
    "v_exp_f32 v129, v129\n\t"
    "v_exp_f32 v130, v130\n\t"
    "v_exp_f32 v131, v131\n\t"
    "v_mfma_f32_32x32x16_bf16 v[144:159], a[36:39], a[12:15], v[144:159]\n\t"
    "ds_read_b128 a[44:47], %10 offset:5120\n\t"
    "v_cvt_pk_bf16_f32 v160, v128, v129\n\t"
    "v_exp_f32 v132, v132\n\t"
    "v_exp_f32 v133, v133\n\t"
    "v_cvt_pk_bf16_f32 v161, v130, v131\n\t"
    "v_exp_f32 v134, v134\n\t"
    "v_mfma_f32_32x32x16_bf16 %3, a[56:59], v[168:171], %3\n\t"
    "v_exp_f32 v135, v135\n\t"
    "v_cvt_pk_bf16_f32 v162, v132, v133\n\t"
    "v_exp_f32 v136, v136\n\t"
    "v_exp_f32 v137, v137\n\t"
    "v_cvt_pk_bf16_f32 v163, v134, v135\n\t"
    "v_mfma_f32_32x32x16_bf16 %3, a[60:63], v[172:175], %3\n\t"
    "v_exp_f32 v138, v138\n\t"
    "v_exp_f32 v139, v139\n\t"
    "v_cvt_pk_bf16_f32 v164, v136, v137\n\t"
    "v_exp_f32 v140, v140\n\t"
    "v_smfmac_f32_16x16x64_bf16 %7, %13, v[168:175], %14\n\t"
    "v_exp_f32 v141, v141\n\t"
    "v_cvt_pk_bf16_f32 v165, v138, v139\n\t"
    "v_exp_f32 v142, v142\n\t"
    "v_exp_f32 v143, v143\n\t"
    "v_cvt_pk_bf16_f32 v166, v140, v141\n\t"
    "v_cvt_pk_bf16_f32 v167, v142, v143\n\t"
    "v_mfma_f32_32x32x16_bf16 v[128:143], a[32:35], a[16:19], 0\n\t"
    "ds_read_b128 a[56:59], %10 offset:6144\n\t"
    "v_exp_f32 v144, v144\n\t"
    "v_exp_f32 v145, v145\n\t"
    "v_exp_f32 v146, v146\n\t"
    "v_exp_f32 v147, v147\n\t"
    "v_mfma_f32_32x32x16_bf16 v[128:143], a[36:39], a[20:23], v[128:143]\n\t"
    "ds_read_b128 a[60:63], %10 offset:7168\n\t"
    "v_cvt_pk_bf16_f32 v168, v144, v145\n\t"
    "v_exp_f32 v148, v148\n\t"
    "v_exp_f32 v149, v149\n\t"
    "v_cvt_pk_bf16_f32 v169, v146, v147\n\t"
    "v_exp_f32 v150, v150\n\t"
    "v_mfma_f32_32x32x16_bf16 %0, a[48:51], v[160:163], %0\n\t"
    "v_exp_f32 v151, v151\n\t"
    "v_cvt_pk_bf16_f32 v170, v148, v149\n\t"
    "v_exp_f32 v152, v152\n\t"
    "v_exp_f32 v153, v153\n\t"
    "v_cvt_pk_bf16_f32 v171, v150, v151\n\t"
    "v_mfma_f32_32x32x16_bf16 %0, a[52:55], v[164:167], %0\n\t"
    "v_exp_f32 v154, v154\n\t"
    "v_exp_f32 v155, v155\n\t"
    "v_cvt_pk_bf16_f32 v172, v152, v153\n\t"
    "v_exp_f32 v156, v156\n\t"
    "v_smfmac_f32_16x16x64_bf16 %4, %13, v[160:167], %14\n\t"
    "v_exp_f32 v157, v157\n\t"
    "v_cvt_pk_bf16_f32 v173, v154, v155\n\t"
    "v_exp_f32 v158, v158\n\t"
    "v_exp_f32 v159, v159\n\t"
    "v_cvt_pk_bf16_f32 v174, v156, v157\n\t"
    "v_cvt_pk_bf16_f32 v175, v158, v159\n\t"
    "v_mfma_f32_32x32x16_bf16 v[144:159], a[32:35], a[24:27], 0\n\t"
    "v_exp_f32 v128, v128\n\t"
    "v_exp_f32 v129, v129\n\t"
    "v_exp_f32 v130, v130\n\t"
    "v_exp_f32 v131, v131\n\t"
    "v_mfma_f32_32x32x16_bf16 v[144:159], a[36:39], a[28:31], v[144:159]\n\t"
    "v_cvt_pk_bf16_f32 v160, v128, v129\n\t"
    "v_exp_f32 v132, v132\n\t"
    "v_exp_f32 v133, v133\n\t"
    "v_cvt_pk_bf16_f32 v161, v130, v131\n\t"
    "v_exp_f32 v134, v134\n\t"
    "v_mfma_f32_32x32x16_bf16 %1, a[48:51], v[168:171], %1\n\t"
    "v_exp_f32 v135, v135\n\t"
    "v_cvt_pk_bf16_f32 v162, v132, v133\n\t"
    "v_exp_f32 v136, v136\n\t"
    "v_exp_f32 v137, v137\n\t"
    "v_cvt_pk_bf16_f32 v163, v134, v135\n\t"
    "v_mfma_f32_32x32x16_bf16 %1, a[52:55], v[172:175], %1\n\t"
    "v_exp_f32 v138, v138\n\t"
    "v_exp_f32 v139, v139\n\t"
    "v_cvt_pk_bf16_f32 v164, v136, v137\n\t"
    "v_exp_f32 v140, v140\n\t"
    "v_smfmac_f32_16x16x64_bf16 %5, %13, v[168:175], %14\n\t"
    "v_exp_f32 v141, v141\n\t"
    "v_cvt_pk_bf16_f32 v165, v138, v139\n\t"
    "v_exp_f32 v142, v142\n\t"
    "v_exp_f32 v143, v143\n\t"
    "v_cvt_pk_bf16_f32 v166, v140, v141\n\t"
    "v_cvt_pk_bf16_f32 v167, v142, v143\n\t"
    "s_waitcnt lgkmcnt(0)\n\t"
    "v_mfma_f32_32x32x16_bf16 v[128:143], a[40:43], a[0:3], 0\n\t"
    "v_exp_f32 v144, v144\n\t"
    "v_exp_f32 v145, v145\n\t"
    "v_exp_f32 v146, v146\n\t"
    "v_exp_f32 v147, v147\n\t"
    "v_mfma_f32_32x32x16_bf16 v[128:143], a[44:47], a[4:7], v[128:143]\n\t"
    "v_cvt_pk_bf16_f32 v168, v144, v145\n\t"
    "v_exp_f32 v148, v148\n\t"
    "v_exp_f32 v149, v149\n\t"
    "v_cvt_pk_bf16_f32 v169, v146, v147\n\t"
    "v_exp_f32 v150, v150\n\t"
    "v_mfma_f32_32x32x16_bf16 %2, a[48:51], v[160:163], %2\n\t"
    "v_exp_f32 v151, v151\n\t"
    "v_cvt_pk_bf16_f32 v170, v148, v149\n\t"
    "v_exp_f32 v152, v152\n\t"
    "v_exp_f32 v153, v153\n\t"
    "v_cvt_pk_bf16_f32 v171, v150, v151\n\t"
    "v_mfma_f32_32x32x16_bf16 %2, a[52:55], v[164:167], %2\n\t"
    "v_exp_f32 v154, v154\n\t"
    "v_exp_f32 v155, v155\n\t"
    "v_cvt_pk_bf16_f32 v172, v152, v153\n\t"
    "v_exp_f32 v156, v156\n\t"
    "v_smfmac_f32_16x16x64_bf16 %6, %13, v[160:167], %14\n\t"
    "v_exp_f32 v157, v157\n\t"
    "v_cvt_pk_bf16_f32 v173, v154, v155\n\t"
    "v_exp_f32 v158, v158\n\t"
    "v_exp_f32 v159, v159\n\t"
    "v_cvt_pk_bf16_f32 v174, v156, v157\n\t"
    "v_cvt_pk_bf16_f32 v175, v158, v159\n\t"
    "v_mfma_f32_32x32x16_bf16 v[144:159], a[40:43], a[8:11], 0\n\t"
    "ds_read_b128 a[32:35], %10 offset:8192\n\t"
    "v_exp_f32 v128, v128\n\t"
    "v_exp_f32 v129, v129\n\t"
    "v_exp_f32 v130, v130\n\t"
    "v_exp_f32 v131, v131\n\t"
    "v_mfma_f32_32x32x16_bf16 v[144:159], a[44:47], a[12:15], v[144:159]\n\t"
    "ds_read_b128 a[36:39], %10 offset:9216\n\t"
    "v_cvt_pk_bf16_f32 v160, v128, v129\n\t"
    "v_exp_f32 v132, v132\n\t"
    "v_exp_f32 v133, v133\n\t"
    "v_cvt_pk_bf16_f32 v161, v130, v131\n\t"
    "v_exp_f32 v134, v134\n\t"
    "v_mfma_f32_32x32x16_bf16 %3, a[48:51], v[168:171], %3\n\t"
    "v_exp_f32 v135, v135\n\t"
    "v_cvt_pk_bf16_f32 v162, v132, v133\n\t"
    "v_exp_f32 v136, v136\n\t"
    "v_exp_f32 v137, v137\n\t"
    "v_cvt_pk_bf16_f32 v163, v134, v135\n\t"
    "v_mfma_f32_32x32x16_bf16 %3, a[52:55], v[172:175], %3\n\t"
    "v_exp_f32 v138, v138\n\t"
    "v_exp_f32 v139, v139\n\t"
    "v_cvt_pk_bf16_f32 v164, v136, v137\n\t"
    "v_exp_f32 v140, v140\n\t"
    "v_smfmac_f32_16x16x64_bf16 %7, %13, v[168:175], %14\n\t"
    "v_exp_f32 v141, v141\n\t"
    "v_cvt_pk_bf16_f32 v165, v138, v139\n\t"
    "v_exp_f32 v142, v142\n\t"
    "v_exp_f32 v143, v143\n\t"
    "v_cvt_pk_bf16_f32 v166, v140, v141\n\t"
    "v_cvt_pk_bf16_f32 v167, v142, v143\n\t"
    "v_mfma_f32_32x32x16_bf16 v[128:143], a[40:43], a[16:19], 0\n\t"
    "ds_read_b128 a[48:51], %10 offset:10240\n\t"
    "v_exp_f32 v144, v144\n\t"
    "v_exp_f32 v145, v145\n\t"
    "v_exp_f32 v146, v146\n\t"
    "v_exp_f32 v147, v147\n\t"
    "v_mfma_f32_32x32x16_bf16 v[128:143], a[44:47], a[20:23], v[128:143]\n\t"
    "ds_read_b128 a[52:55], %10 offset:11264\n\t"
    "v_cvt_pk_bf16_f32 v168, v144, v145\n\t"
    "v_exp_f32 v148, v148\n\t"
    "v_exp_f32 v149, v149\n\t"
    "v_cvt_pk_bf16_f32 v169, v146, v147\n\t"
    "v_exp_f32 v150, v150\n\t"
    "v_mfma_f32_32x32x16_bf16 %0, a[56:59], v[160:163], %0\n\t"
    "v_exp_f32 v151, v151\n\t"
    "v_cvt_pk_bf16_f32 v170, v148, v149\n\t"
    "v_exp_f32 v152, v152\n\t"
    "v_exp_f32 v153, v153\n\t"
    "v_cvt_pk_bf16_f32 v171, v150, v151\n\t"
    "v_mfma_f32_32x32x16_bf16 %0, a[60:63], v[164:167], %0\n\t"
    "v_exp_f32 v154, v154\n\t"
    "v_exp_f32 v155, v155\n\t"
    "v_cvt_pk_bf16_f32 v172, v152, v153\n\t"
    "v_exp_f32 v156, v156\n\t"
    "v_smfmac_f32_16x16x64_bf16 %4, %13, v[160:167], %14\n\t"
    "v_exp_f32 v157, v157\n\t"
    "v_cvt_pk_bf16_f32 v173, v154, v155\n\t"
    "v_exp_f32 v158, v158\n\t"
    "v_exp_f32 v159, v159\n\t"
    "v_cvt_pk_bf16_f32 v174, v156, v157\n\t"
    "v_cvt_pk_bf16_f32 v175, v158, v159\n\t"
    "v_mfma_f32_32x32x16_bf16 v[144:159], a[40:43], a[24:27], 0\n\t"
    "v_exp_f32 v128, v128\n\t"
    "v_exp_f32 v129, v129\n\t"
    "v_exp_f32 v130, v130\n\t"
    "v_exp_f32 v131, v131\n\t"
    "v_mfma_f32_32x32x16_bf16 v[144:159], a[44:47], a[28:31], v[144:159]\n\t"
    "v_cvt_pk_bf16_f32 v160, v128, v129\n\t"
    "v_exp_f32 v132, v132\n\t"
    "v_exp_f32 v133, v133\n\t"
    "v_cvt_pk_bf16_f32 v161, v130, v131\n\t"
    "v_exp_f32 v134, v134\n\t"
    "v_mfma_f32_32x32x16_bf16 %1, a[56:59], v[168:171], %1\n\t"
    "v_exp_f32 v135, v135\n\t"
    "v_cvt_pk_bf16_f32 v162, v132, v133\n\t"
    "v_exp_f32 v136, v136\n\t"
    "v_exp_f32 v137, v137\n\t"
    "v_cvt_pk_bf16_f32 v163, v134, v135\n\t"
    "v_mfma_f32_32x32x16_bf16 %1, a[60:63], v[172:175], %1\n\t"
    "v_exp_f32 v138, v138\n\t"
    "v_exp_f32 v139, v139\n\t"
    "v_cvt_pk_bf16_f32 v164, v136, v137\n\t"
    "v_exp_f32 v140, v140\n\t"
    "v_smfmac_f32_16x16x64_bf16 %5, %13, v[168:175], %14\n\t"
    "v_exp_f32 v141, v141\n\t"
    "v_cvt_pk_bf16_f32 v165, v138, v139\n\t"
    "v_exp_f32 v142, v142\n\t"
    "v_exp_f32 v143, v143\n\t"
    "v_cvt_pk_bf16_f32 v166, v140, v141\n\t"
    "v_cvt_pk_bf16_f32 v167, v142, v143\n\t"
    "s_waitcnt lgkmcnt(0)\n\t"
    "v_mfma_f32_32x32x16_bf16 v[128:143], a[32:35], a[0:3], 0\n\t"
    "v_exp_f32 v144, v144\n\t"
    "v_exp_f32 v145, v145\n\t"
    "v_exp_f32 v146, v146\n\t"
    "v_exp_f32 v147, v147\n\t"
    "v_mfma_f32_32x32x16_bf16 v[128:143], a[36:39], a[4:7], v[128:143]\n\t"
    "v_cvt_pk_bf16_f32 v168, v144, v145\n\t"
    "v_exp_f32 v148, v148\n\t"
    "v_exp_f32 v149, v149\n\t"
    "v_cvt_pk_bf16_f32 v169, v146, v147\n\t"
    "v_exp_f32 v150, v150\n\t"
    "v_mfma_f32_32x32x16_bf16 %2, a[56:59], v[160:163], %2\n\t"
    "v_exp_f32 v151, v151\n\t"
    "v_cvt_pk_bf16_f32 v170, v148, v149\n\t"
    "v_exp_f32 v152, v152\n\t"
    "v_exp_f32 v153, v153\n\t"
    "v_cvt_pk_bf16_f32 v171, v150, v151\n\t"
    "v_mfma_f32_32x32x16_bf16 %2, a[60:63], v[164:167], %2\n\t"
    "v_exp_f32 v154, v154\n\t"
    "v_exp_f32 v155, v155\n\t"
    "v_cvt_pk_bf16_f32 v172, v152, v153\n\t"
    "v_exp_f32 v156, v156\n\t"
    "v_smfmac_f32_16x16x64_bf16 %6, %13, v[160:167], %14\n\t"
    "v_exp_f32 v157, v157\n\t"
    "v_cvt_pk_bf16_f32 v173, v154, v155\n\t"
    "v_exp_f32 v158, v158\n\t"
    "v_exp_f32 v159, v159\n\t"
    "v_cvt_pk_bf16_f32 v174, v156, v157\n\t"
    "v_cvt_pk_bf16_f32 v175, v158, v159\n\t"
    "v_mfma_f32_32x32x16_bf16 v[144:159], a[32:35], a[8:11], 0\n\t"
    "ds_read_b128 a[40:43], %10 offset:12288\n\t"
    "v_exp_f32 v128, v128\n\t"
    "v_exp_f32 v129, v129\n\t"
    "v_exp_f32 v130, v130\n\t"
    "v_exp_f32 v131, v131\n\t"
    "v_mfma_f32_32x32x16_bf16 v[144:159], a[36:39], a[12:15], v[144:159]\n\t"
    "ds_read_b128 a[44:47], %10 offset:13312\n\t"
    "v_cvt_pk_bf16_f32 v160, v128, v129\n\t"
    "v_exp_f32 v132, v132\n\t"
    "v_exp_f32 v133, v133\n\t"
    "v_cvt_pk_bf16_f32 v161, v130, v131\n\t"
    "v_exp_f32 v134, v134\n\t"
    "v_mfma_f32_32x32x16_bf16 %3, a[56:59], v[168:171], %3\n\t"
    "v_exp_f32 v135, v135\n\t"
    "v_cvt_pk_bf16_f32 v162, v132, v133\n\t"
    "v_exp_f32 v136, v136\n\t"
    "v_exp_f32 v137, v137\n\t"
    "v_cvt_pk_bf16_f32 v163, v134, v135\n\t"
    "v_mfma_f32_32x32x16_bf16 %3, a[60:63], v[172:175], %3\n\t"
    "v_exp_f32 v138, v138\n\t"
    "v_exp_f32 v139, v139\n\t"
    "v_cvt_pk_bf16_f32 v164, v136, v137\n\t"
    "v_exp_f32 v140, v140\n\t"
    "v_smfmac_f32_16x16x64_bf16 %7, %13, v[168:175], %14\n\t"
    "v_exp_f32 v141, v141\n\t"
    "v_cvt_pk_bf16_f32 v165, v138, v139\n\t"
    "v_exp_f32 v142, v142\n\t"
    "v_exp_f32 v143, v143\n\t"
    "v_cvt_pk_bf16_f32 v166, v140, v141\n\t"
    "v_cvt_pk_bf16_f32 v167, v142, v143\n\t"
    "v_mfma_f32_32x32x16_bf16 v[128:143], a[32:35], a[16:19], 0\n\t"
    "ds_read_b128 a[56:59], %10 offset:14336\n\t"
    "v_exp_f32 v144, v144\n\t"
    "v_exp_f32 v145, v145\n\t"
    "v_exp_f32 v146, v146\n\t"
    "v_exp_f32 v147, v147\n\t"
    "v_mfma_f32_32x32x16_bf16 v[128:143], a[36:39], a[20:23], v[128:143]\n\t"
    "ds_read_b128 a[60:63], %10 offset:15360\n\t"
    "v_cvt_pk_bf16_f32 v168, v144, v145\n\t"
    "v_exp_f32 v148, v148\n\t"
    "v_exp_f32 v149, v149\n\t"
    "v_cvt_pk_bf16_f32 v169, v146, v147\n\t"
    "v_exp_f32 v150, v150\n\t"
    "v_mfma_f32_32x32x16_bf16 %0, a[48:51], v[160:163], %0\n\t"
    "v_exp_f32 v151, v151\n\t"
    "v_cvt_pk_bf16_f32 v170, v148, v149\n\t"
    "v_exp_f32 v152, v152\n\t"
    "v_exp_f32 v153, v153\n\t"
    "v_cvt_pk_bf16_f32 v171, v150, v151\n\t"
    "v_mfma_f32_32x32x16_bf16 %0, a[52:55], v[164:167], %0\n\t"
    "v_exp_f32 v154, v154\n\t"
    "v_exp_f32 v155, v155\n\t"
    "v_cvt_pk_bf16_f32 v172, v152, v153\n\t"
    "v_exp_f32 v156, v156\n\t"
    "v_smfmac_f32_16x16x64_bf16 %4, %13, v[160:167], %14\n\t"
    "v_exp_f32 v157, v157\n\t"
    "v_cvt_pk_bf16_f32 v173, v154, v155\n\t"
    "v_exp_f32 v158, v158\n\t"
    "v_exp_f32 v159, v159\n\t"
    "v_cvt_pk_bf16_f32 v174, v156, v157\n\t"
    "v_cvt_pk_bf16_f32 v175, v158, v159\n\t"
    "v_mfma_f32_32x32x16_bf16 v[144:159], a[32:35], a[24:27], 0\n\t"
    "v_exp_f32 v128, v128\n\t"
    "v_exp_f32 v129, v129\n\t"
    "v_exp_f32 v130, v130\n\t"
    "v_exp_f32 v131, v131\n\t"
    "v_mfma_f32_32x32x16_bf16 v[144:159], a[36:39], a[28:31], v[144:159]\n\t"
    "v_cvt_pk_bf16_f32 v160, v128, v129\n\t"
    "v_exp_f32 v132, v132\n\t"
    "v_exp_f32 v133, v133\n\t"
    "v_cvt_pk_bf16_f32 v161, v130, v131\n\t"
    "v_exp_f32 v134, v134\n\t"
    "v_mfma_f32_32x32x16_bf16 %1, a[48:51], v[168:171], %1\n\t"
    "v_exp_f32 v135, v135\n\t"
    "v_cvt_pk_bf16_f32 v162, v132, v133\n\t"
    "v_exp_f32 v136, v136\n\t"
    "v_exp_f32 v137, v137\n\t"
    "v_cvt_pk_bf16_f32 v163, v134, v135\n\t"
    "v_mfma_f32_32x32x16_bf16 %1, a[52:55], v[172:175], %1\n\t"
    "v_exp_f32 v138, v138\n\t"
    "v_exp_f32 v139, v139\n\t"
    "v_cvt_pk_bf16_f32 v164, v136, v137\n\t"
    "v_exp_f32 v140, v140\n\t"
    "v_smfmac_f32_16x16x64_bf16 %5, %13, v[168:175], %14\n\t"
    "v_exp_f32 v141, v141\n\t"
    "v_cvt_pk_bf16_f32 v165, v138, v139\n\t"
    "v_exp_f32 v142, v142\n\t"
    "v_exp_f32 v143, v143\n\t"
    "v_cvt_pk_bf16_f32 v166, v140, v141\n\t"
    "v_cvt_pk_bf16_f32 v167, v142, v143\n\t"
    "s_waitcnt lgkmcnt(0)\n\t"
    "v_mfma_f32_32x32x16_bf16 v[128:143], a[40:43], a[0:3], 0\n\t"
    "v_exp_f32 v144, v144\n\t"
    "v_exp_f32 v145, v145\n\t"
    "v_exp_f32 v146, v146\n\t"
    "v_exp_f32 v147, v147\n\t"
    "v_mfma_f32_32x32x16_bf16 v[128:143], a[44:47], a[4:7], v[128:143]\n\t"
    "v_cvt_pk_bf16_f32 v168, v144, v145\n\t"
    "v_exp_f32 v148, v148\n\t"
    "v_exp_f32 v149, v149\n\t"
    "v_cvt_pk_bf16_f32 v169, v146, v147\n\t"
    "v_exp_f32 v150, v150\n\t"
    "v_mfma_f32_32x32x16_bf16 %2, a[48:51], v[160:163], %2\n\t"
    "v_exp_f32 v151, v151\n\t"
    "v_cvt_pk_bf16_f32 v170, v148, v149\n\t"
    "v_exp_f32 v152, v152\n\t"
    "v_exp_f32 v153, v153\n\t"
    "v_cvt_pk_bf16_f32 v171, v150, v151\n\t"
    "v_mfma_f32_32x32x16_bf16 %2, a[52:55], v[164:167], %2\n\t"
    "v_exp_f32 v154, v154\n\t"
    "v_exp_f32 v155, v155\n\t"
    "v_cvt_pk_bf16_f32 v172, v152, v153\n\t"
    "v_exp_f32 v156, v156\n\t"
    "v_smfmac_f32_16x16x64_bf16 %6, %13, v[160:167], %14\n\t"
    "v_exp_f32 v157, v157\n\t"
    "v_cvt_pk_bf16_f32 v173, v154, v155\n\t"
    "v_exp_f32 v158, v158\n\t"
    "v_exp_f32 v159, v159\n\t"
    "v_cvt_pk_bf16_f32 v174, v156, v157\n\t"
    "v_cvt_pk_bf16_f32 v175, v158, v159\n\t"
    "v_mfma_f32_32x32x16_bf16 v[144:159], a[40:43], a[8:11], 0\n\t"
    "s_barrier\n\t"
    "ds_read_b128 a[32:35], %10 offset:16384\n\t"
    "v_exp_f32 v128, v128\n\t"
    "v_exp_f32 v129, v129\n\t"
    "v_exp_f32 v130, v130\n\t"
    "v_exp_f32 v131, v131\n\t"
    "v_mfma_f32_32x32x16_bf16 v[144:159], a[44:47], a[12:15], v[144:159]\n\t"
    "ds_read_b128 a[36:39], %10 offset:17408\n\t"
    "v_cvt_pk_bf16_f32 v160, v128, v129\n\t"
    "v_exp_f32 v132, v132\n\t"
    "v_exp_f32 v133, v133\n\t"
    "v_cvt_pk_bf16_f32 v161, v130, v131\n\t"
    "v_exp_f32 v134, v134\n\t"
    "v_mfma_f32_32x32x16_bf16 %3, a[48:51], v[168:171], %3\n\t"
    "v_exp_f32 v135, v135\n\t"
    "v_cvt_pk_bf16_f32 v162, v132, v133\n\t"
    "v_exp_f32 v136, v136\n\t"
    "v_exp_f32 v137, v137\n\t"
    "v_cvt_pk_bf16_f32 v163, v134, v135\n\t"
    "v_mfma_f32_32x32x16_bf16 %3, a[52:55], v[172:175], %3\n\t"
    "v_exp_f32 v138, v138\n\t"
    "v_exp_f32 v139, v139\n\t"
    "v_cvt_pk_bf16_f32 v164, v136, v137\n\t"
    "v_exp_f32 v140, v140\n\t"
    "v_smfmac_f32_16x16x64_bf16 %7, %13, v[168:175], %14\n\t"
    "v_exp_f32 v141, v141\n\t"
    "v_cvt_pk_bf16_f32 v165, v138, v139\n\t"
    "v_exp_f32 v142, v142\n\t"
    "v_exp_f32 v143, v143\n\t"
    "v_cvt_pk_bf16_f32 v166, v140, v141\n\t"
    "v_cvt_pk_bf16_f32 v167, v142, v143\n\t"
    "v_mfma_f32_32x32x16_bf16 v[128:143], a[40:43], a[16:19], 0\n\t"
    "ds_read_b128 a[48:51], %10 offset:18432\n\t"
    "v_exp_f32 v144, v144\n\t"
    "v_exp_f32 v145, v145\n\t"
    "v_exp_f32 v146, v146\n\t"
    "v_exp_f32 v147, v147\n\t"
    "v_mfma_f32_32x32x16_bf16 v[128:143], a[44:47], a[20:23], v[128:143]\n\t"
    "ds_read_b128 a[52:55], %10 offset:19456\n\t"
    "v_cvt_pk_bf16_f32 v168, v144, v145\n\t"
    "v_exp_f32 v148, v148\n\t"
    "v_exp_f32 v149, v149\n\t"
    "v_cvt_pk_bf16_f32 v169, v146, v147\n\t"
    "v_exp_f32 v150, v150\n\t"
    "v_mfma_f32_32x32x16_bf16 %0, a[56:59], v[160:163], %0\n\t"
    "v_exp_f32 v151, v151\n\t"
    "v_cvt_pk_bf16_f32 v170, v148, v149\n\t"
    "v_exp_f32 v152, v152\n\t"
    "v_exp_f32 v153, v153\n\t"
    "v_cvt_pk_bf16_f32 v171, v150, v151\n\t"
    "v_mfma_f32_32x32x16_bf16 %0, a[60:63], v[164:167], %0\n\t"
    "v_exp_f32 v154, v154\n\t"
    "v_exp_f32 v155, v155\n\t"
    "v_cvt_pk_bf16_f32 v172, v152, v153\n\t"
    "v_exp_f32 v156, v156\n\t"
    "v_smfmac_f32_16x16x64_bf16 %4, %13, v[160:167], %14\n\t"
    "v_exp_f32 v157, v157\n\t"
    "v_cvt_pk_bf16_f32 v173, v154, v155\n\t"
    "v_exp_f32 v158, v158\n\t"
    "v_exp_f32 v159, v159\n\t"
    "v_cvt_pk_bf16_f32 v174, v156, v157\n\t"
    "v_cvt_pk_bf16_f32 v175, v158, v159\n\t"
    "v_mfma_f32_32x32x16_bf16 v[144:159], a[40:43], a[24:27], 0\n\t"
    "v_exp_f32 v128, v128\n\t"
    "v_exp_f32 v129, v129\n\t"
    "v_exp_f32 v130, v130\n\t"
    "v_exp_f32 v131, v131\n\t"
    "v_mfma_f32_32x32x16_bf16 v[144:159], a[44:47], a[28:31], v[144:159]\n\t"
    "v_cvt_pk_bf16_f32 v160, v128, v129\n\t"
    "v_exp_f32 v132, v132\n\t"
    "v_exp_f32 v133, v133\n\t"
    "v_cvt_pk_bf16_f32 v161, v130, v131\n\t"
    "v_exp_f32 v134, v134\n\t"
    "v_mfma_f32_32x32x16_bf16 %1, a[56:59], v[168:171], %1\n\t"
    "v_exp_f32 v135, v135\n\t"
    "v_cvt_pk_bf16_f32 v162, v132, v133\n\t"
    "v_exp_f32 v136, v136\n\t"
    "v_exp_f32 v137, v137\n\t"
    "v_cvt_pk_bf16_f32 v163, v134, v135\n\t"
    "v_mfma_f32_32x32x16_bf16 %1, a[60:63], v[172:175], %1\n\t"
    "v_exp_f32 v138, v138\n\t"
    "v_exp_f32 v139, v139\n\t"
    "v_cvt_pk_bf16_f32 v164, v136, v137\n\t"
    "v_exp_f32 v140, v140\n\t"
    "v_smfmac_f32_16x16x64_bf16 %5, %13, v[168:175], %14\n\t"
    "v_exp_f32 v141, v141\n\t"
    "v_cvt_pk_bf16_f32 v165, v138, v139\n\t"
    "v_exp_f32 v142, v142\n\t"
    "v_exp_f32 v143, v143\n\t"
    "v_cvt_pk_bf16_f32 v166, v140, v141\n\t"
    "v_cvt_pk_bf16_f32 v167, v142, v143\n\t"
    "s_waitcnt lgkmcnt(0)\n\t"
    "v_mfma_f32_32x32x16_bf16 v[128:143], a[32:35], a[0:3], 0\n\t"
    "v_exp_f32 v144, v144\n\t"
    "v_exp_f32 v145, v145\n\t"
    "v_exp_f32 v146, v146\n\t"
    "v_exp_f32 v147, v147\n\t"
    "v_mfma_f32_32x32x16_bf16 v[128:143], a[36:39], a[4:7], v[128:143]\n\t"
    "v_cvt_pk_bf16_f32 v168, v144, v145\n\t"
    "v_exp_f32 v148, v148\n\t"
    "v_exp_f32 v149, v149\n\t"
    "v_cvt_pk_bf16_f32 v169, v146, v147\n\t"
    "v_exp_f32 v150, v150\n\t"
    "v_mfma_f32_32x32x16_bf16 %2, a[56:59], v[160:163], %2\n\t"
    "v_exp_f32 v151, v151\n\t"
    "v_cvt_pk_bf16_f32 v170, v148, v149\n\t"
    "v_exp_f32 v152, v152\n\t"
    "v_exp_f32 v153, v153\n\t"
    "v_cvt_pk_bf16_f32 v171, v150, v151\n\t"
    "v_mfma_f32_32x32x16_bf16 %2, a[60:63], v[164:167], %2\n\t"
    "v_exp_f32 v154, v154\n\t"
    "v_exp_f32 v155, v155\n\t"
    "v_cvt_pk_bf16_f32 v172, v152, v153\n\t"
    "v_exp_f32 v156, v156\n\t"
    "v_smfmac_f32_16x16x64_bf16 %6, %13, v[160:167], %14\n\t"
    "v_exp_f32 v157, v157\n\t"
    "v_cvt_pk_bf16_f32 v173, v154, v155\n\t"
    "v_exp_f32 v158, v158\n\t"
    "v_exp_f32 v159, v159\n\t"
    "v_cvt_pk_bf16_f32 v174, v156, v157\n\t"
    "v_cvt_pk_bf16_f32 v175, v158, v159\n\t"
    "v_mfma_f32_32x32x16_bf16 v[144:159], a[32:35], a[8:11], 0\n\t"
    "ds_read_b128 a[40:43], %10 offset:20480\n\t"
    "v_exp_f32 v128, v128\n\t"
    "v_exp_f32 v129, v129\n\t"
    "v_exp_f32 v130, v130\n\t"
    "v_exp_f32 v131, v131\n\t"
    "v_mfma_f32_32x32x16_bf16 v[144:159], a[36:39], a[12:15], v[144:159]\n\t"
    "ds_read_b128 a[44:47], %10 offset:21504\n\t"
    "v_cvt_pk_bf16_f32 v160, v128, v129\n\t"
    "v_exp_f32 v132, v132\n\t"
    "v_exp_f32 v133, v133\n\t"
    "v_cvt_pk_bf16_f32 v161, v130, v131\n\t"
    "v_exp_f32 v134, v134\n\t"
    "v_mfma_f32_32x32x16_bf16 %3, a[56:59], v[168:171], %3\n\t"
    "v_exp_f32 v135, v135\n\t"
    "v_cvt_pk_bf16_f32 v162, v132, v133\n\t"
    "v_exp_f32 v136, v136\n\t"
    "v_exp_f32 v137, v137\n\t"
    "v_cvt_pk_bf16_f32 v163, v134, v135\n\t"
    "v_mfma_f32_32x32x16_bf16 %3, a[60:63], v[172:175], %3\n\t"
    "v_exp_f32 v138, v138\n\t"
    "v_exp_f32 v139, v139\n\t"
    "v_cvt_pk_bf16_f32 v164, v136, v137\n\t"
    "v_exp_f32 v140, v140\n\t"
    "v_smfmac_f32_16x16x64_bf16 %7, %13, v[168:175], %14\n\t"
    "v_exp_f32 v141, v141\n\t"
    "v_cvt_pk_bf16_f32 v165, v138, v139\n\t"
    "v_exp_f32 v142, v142\n\t"
    "v_exp_f32 v143, v143\n\t"
    "v_cvt_pk_bf16_f32 v166, v140, v141\n\t"
    "v_cvt_pk_bf16_f32 v167, v142, v143\n\t"
    "v_mfma_f32_32x32x16_bf16 v[128:143], a[32:35], a[16:19], 0\n\t"
    "ds_read_b128 a[56:59], %10 offset:22528\n\t"
    "v_exp_f32 v144, v144\n\t"
    "v_exp_f32 v145, v145\n\t"
    "v_exp_f32 v146, v146\n\t"
    "v_exp_f32 v147, v147\n\t"
    "v_mfma_f32_32x32x16_bf16 v[128:143], a[36:39], a[20:23], v[128:143]\n\t"
    "ds_read_b128 a[60:63], %10 offset:23552\n\t"
    "v_cvt_pk_bf16_f32 v168, v144, v145\n\t"
    "v_exp_f32 v148, v148\n\t"
    "v_exp_f32 v149, v149\n\t"
    "v_cvt_pk_bf16_f32 v169, v146, v147\n\t"
    "v_exp_f32 v150, v150\n\t"
    "v_mfma_f32_32x32x16_bf16 %0, a[48:51], v[160:163], %0\n\t"
    "v_exp_f32 v151, v151\n\t"
    "v_cvt_pk_bf16_f32 v170, v148, v149\n\t"
    "v_exp_f32 v152, v152\n\t"
    "v_exp_f32 v153, v153\n\t"
    "v_cvt_pk_bf16_f32 v171, v150, v151\n\t"
    "v_mfma_f32_32x32x16_bf16 %0, a[52:55], v[164:167], %0\n\t"
    "v_exp_f32 v154, v154\n\t"
    "v_exp_f32 v155, v155\n\t"
    "v_cvt_pk_bf16_f32 v172, v152, v153\n\t"
    "v_exp_f32 v156, v156\n\t"
    "v_smfmac_f32_16x16x64_bf16 %4, %13, v[160:167], %14\n\t"
    "v_exp_f32 v157, v157\n\t"
    "v_cvt_pk_bf16_f32 v173, v154, v155\n\t"
    "v_exp_f32 v158, v158\n\t"
    "v_exp_f32 v159, v159\n\t"
    "v_cvt_pk_bf16_f32 v174, v156, v157\n\t"
    "v_cvt_pk_bf16_f32 v175, v158, v159\n\t"
    "v_mfma_f32_32x32x16_bf16 v[144:159], a[32:35], a[24:27], 0\n\t"
    "v_exp_f32 v128, v128\n\t"
    "v_exp_f32 v129, v129\n\t"
    "v_exp_f32 v130, v130\n\t"
    "v_exp_f32 v131, v131\n\t"
    "v_mfma_f32_32x32x16_bf16 v[144:159], a[36:39], a[28:31], v[144:159]\n\t"
    "v_cvt_pk_bf16_f32 v160, v128, v129\n\t"
    "v_exp_f32 v132, v132\n\t"
    "v_exp_f32 v133, v133\n\t"
    "v_cvt_pk_bf16_f32 v161, v130, v131\n\t"
    "v_exp_f32 v134, v134\n\t"
    "v_mfma_f32_32x32x16_bf16 %1, a[48:51], v[168:171], %1\n\t"
    "v_exp_f32 v135, v135\n\t"
    "v_cvt_pk_bf16_f32 v162, v132, v133\n\t"
    "v_exp_f32 v136, v136\n\t"
    "v_exp_f32 v137, v137\n\t"
    "v_cvt_pk_bf16_f32 v163, v134, v135\n\t"
    "v_mfma_f32_32x32x16_bf16 %1, a[52:55], v[172:175], %1\n\t"
    "v_exp_f32 v138, v138\n\t"
    "v_exp_f32 v139, v139\n\t"
    "v_cvt_pk_bf16_f32 v164, v136, v137\n\t"
    "v_exp_f32 v140, v140\n\t"
    "v_smfmac_f32_16x16x64_bf16 %5, %13, v[168:175], %14\n\t"
    "v_exp_f32 v141, v141\n\t"
    "v_cvt_pk_bf16_f32 v165, v138, v139\n\t"
    "v_exp_f32 v142, v142\n\t"
    "v_exp_f32 v143, v143\n\t"
    "v_cvt_pk_bf16_f32 v166, v140, v141\n\t"
    "v_cvt_pk_bf16_f32 v167, v142, v143\n\t"
    "s_waitcnt lgkmcnt(0)\n\t"
    "v_mfma_f32_32x32x16_bf16 v[128:143], a[40:43], a[0:3], 0\n\t"
    "v_exp_f32 v144, v144\n\t"
    "v_exp_f32 v145, v145\n\t"
    "v_exp_f32 v146, v146\n\t"
    "v_exp_f32 v147, v147\n\t"
    "v_mfma_f32_32x32x16_bf16 v[128:143], a[44:47], a[4:7], v[128:143]\n\t"
    "v_cvt_pk_bf16_f32 v168, v144, v145\n\t"
    "v_exp_f32 v148, v148\n\t"
    "v_exp_f32 v149, v149\n\t"
    "v_cvt_pk_bf16_f32 v169, v146, v147\n\t"
    "v_exp_f32 v150, v150\n\t"
    "v_mfma_f32_32x32x16_bf16 %2, a[48:51], v[160:163], %2\n\t"
    "v_exp_f32 v151, v151\n\t"
    "v_cvt_pk_bf16_f32 v170, v148, v149\n\t"
    "v_exp_f32 v152, v152\n\t"
    "v_exp_f32 v153, v153\n\t"
    "v_cvt_pk_bf16_f32 v171, v150, v151\n\t"
    "v_mfma_f32_32x32x16_bf16 %2, a[52:55], v[164:167], %2\n\t"
    "v_exp_f32 v154, v154\n\t"
    "v_exp_f32 v155, v155\n\t"
    "v_cvt_pk_bf16_f32 v172, v152, v153\n\t"
    "v_exp_f32 v156, v156\n\t"
    "v_smfmac_f32_16x16x64_bf16 %6, %13, v[160:167], %14\n\t"
    "v_exp_f32 v157, v157\n\t"
    "v_cvt_pk_bf16_f32 v173, v154, v155\n\t"
    "v_exp_f32 v158, v158\n\t"
    "v_exp_f32 v159, v159\n\t"
    "v_cvt_pk_bf16_f32 v174, v156, v157\n\t"
    "v_cvt_pk_bf16_f32 v175, v158, v159\n\t"
    "v_mfma_f32_32x32x16_bf16 v[144:159], a[40:43], a[8:11], 0\n\t"
    "ds_read_b128 a[32:35], %10 offset:24576\n\t"
    "v_exp_f32 v128, v128\n\t"
    "v_exp_f32 v129, v129\n\t"
    "v_exp_f32 v130, v130\n\t"
    "v_exp_f32 v131, v131\n\t"
    "v_mfma_f32_32x32x16_bf16 v[144:159], a[44:47], a[12:15], v[144:159]\n\t"
    "ds_read_b128 a[36:39], %10 offset:25600\n\t"
    "v_cvt_pk_bf16_f32 v160, v128, v129\n\t"
    "v_exp_f32 v132, v132\n\t"
    "v_exp_f32 v133, v133\n\t"
    "v_cvt_pk_bf16_f32 v161, v130, v131\n\t"
    "v_exp_f32 v134, v134\n\t"
    "v_mfma_f32_32x32x16_bf16 %3, a[48:51], v[168:171], %3\n\t"
    "v_exp_f32 v135, v135\n\t"
    "v_cvt_pk_bf16_f32 v162, v132, v133\n\t"
    "v_exp_f32 v136, v136\n\t"
    "v_exp_f32 v137, v137\n\t"
    "v_cvt_pk_bf16_f32 v163, v134, v135\n\t"
    "v_mfma_f32_32x32x16_bf16 %3, a[52:55], v[172:175], %3\n\t"
    "v_exp_f32 v138, v138\n\t"
    "v_exp_f32 v139, v139\n\t"
    "v_cvt_pk_bf16_f32 v164, v136, v137\n\t"
    "v_exp_f32 v140, v140\n\t"
    "v_smfmac_f32_16x16x64_bf16 %7, %13, v[168:175], %14\n\t"
    "v_exp_f32 v141, v141\n\t"
    "v_cvt_pk_bf16_f32 v165, v138, v139\n\t"
    "v_exp_f32 v142, v142\n\t"
    "v_exp_f32 v143, v143\n\t"
    "v_cvt_pk_bf16_f32 v166, v140, v141\n\t"
    "v_cvt_pk_bf16_f32 v167, v142, v143\n\t"
    "v_mfma_f32_32x32x16_bf16 v[128:143], a[40:43], a[16:19], 0\n\t"
    "ds_read_b128 a[48:51], %10 offset:26624\n\t"
    "v_exp_f32 v144, v144\n\t"
    "v_exp_f32 v145, v145\n\t"
    "v_exp_f32 v146, v146\n\t"
    "v_exp_f32 v147, v147\n\t"
    "v_mfma_f32_32x32x16_bf16 v[128:143], a[44:47], a[20:23], v[128:143]\n\t"
    "ds_read_b128 a[52:55], %10 offset:27648\n\t"
    "v_cvt_pk_bf16_f32 v168, v144, v145\n\t"
    "v_exp_f32 v148, v148\n\t"
    "v_exp_f32 v149, v149\n\t"
    "v_cvt_pk_bf16_f32 v169, v146, v147\n\t"
    "v_exp_f32 v150, v150\n\t"
    "v_mfma_f32_32x32x16_bf16 %0, a[56:59], v[160:163], %0\n\t"
    "v_exp_f32 v151, v151\n\t"
    "v_cvt_pk_bf16_f32 v170, v148, v149\n\t"
    "v_exp_f32 v152, v152\n\t"
    "v_exp_f32 v153, v153\n\t"
    "v_cvt_pk_bf16_f32 v171, v150, v151\n\t"
    "v_mfma_f32_32x32x16_bf16 %0, a[60:63], v[164:167], %0\n\t"
    "v_exp_f32 v154, v154\n\t"
    "v_exp_f32 v155, v155\n\t"
    "v_cvt_pk_bf16_f32 v172, v152, v153\n\t"
    "v_exp_f32 v156, v156\n\t"
    "v_smfmac_f32_16x16x64_bf16 %4, %13, v[160:167], %14\n\t"
    "v_exp_f32 v157, v157\n\t"
    "v_cvt_pk_bf16_f32 v173, v154, v155\n\t"
    "v_exp_f32 v158, v158\n\t"
    "v_exp_f32 v159, v159\n\t"
    "v_cvt_pk_bf16_f32 v174, v156, v157\n\t"
    "v_cvt_pk_bf16_f32 v175, v158, v159\n\t"
    "v_mfma_f32_32x32x16_bf16 v[144:159], a[40:43], a[24:27], 0\n\t"
    "v_exp_f32 v128, v128\n\t"
    "v_exp_f32 v129, v129\n\t"
    "v_exp_f32 v130, v130\n\t"
    "v_exp_f32 v131, v131\n\t"
    "v_mfma_f32_32x32x16_bf16 v[144:159], a[44:47], a[28:31], v[144:159]\n\t"
    "v_cvt_pk_bf16_f32 v160, v128, v129\n\t"
    "v_exp_f32 v132, v132\n\t"
    "v_exp_f32 v133, v133\n\t"
    "v_cvt_pk_bf16_f32 v161, v130, v131\n\t"
    "v_exp_f32 v134, v134\n\t"
    "v_mfma_f32_32x32x16_bf16 %1, a[56:59], v[168:171], %1\n\t"
    "v_exp_f32 v135, v135\n\t"
    "v_cvt_pk_bf16_f32 v162, v132, v133\n\t"
    "v_exp_f32 v136, v136\n\t"
    "v_exp_f32 v137, v137\n\t"
    "v_cvt_pk_bf16_f32 v163, v134, v135\n\t"
    "v_mfma_f32_32x32x16_bf16 %1, a[60:63], v[172:175], %1\n\t"
    "v_exp_f32 v138, v138\n\t"
    "v_exp_f32 v139, v139\n\t"
    "v_cvt_pk_bf16_f32 v164, v136, v137\n\t"
    "v_exp_f32 v140, v140\n\t"
    "v_smfmac_f32_16x16x64_bf16 %5, %13, v[168:175], %14\n\t"
    "v_exp_f32 v141, v141\n\t"
    "v_cvt_pk_bf16_f32 v165, v138, v139\n\t"
    "v_exp_f32 v142, v142\n\t"
    "v_exp_f32 v143, v143\n\t"
    "v_cvt_pk_bf16_f32 v166, v140, v141\n\t"
    "v_cvt_pk_bf16_f32 v167, v142, v143\n\t"
    "s_waitcnt lgkmcnt(0)\n\t"
    "v_mfma_f32_32x32x16_bf16 v[128:143], a[32:35], a[0:3], 0\n\t"
    "v_exp_f32 v144, v144\n\t"
    "v_exp_f32 v145, v145\n\t"
    "v_exp_f32 v146, v146\n\t"
    "v_exp_f32 v147, v147\n\t"
    "v_mfma_f32_32x32x16_bf16 v[128:143], a[36:39], a[4:7], v[128:143]\n\t"
    "v_cvt_pk_bf16_f32 v168, v144, v145\n\t"
    "v_exp_f32 v148, v148\n\t"
    "v_exp_f32 v149, v149\n\t"
    "v_cvt_pk_bf16_f32 v169, v146, v147\n\t"
    "v_exp_f32 v150, v150\n\t"
    "v_mfma_f32_32x32x16_bf16 %2, a[56:59], v[160:163], %2\n\t"
    "v_exp_f32 v151, v151\n\t"
    "v_cvt_pk_bf16_f32 v170, v148, v149\n\t"
    "v_exp_f32 v152, v152\n\t"
    "v_exp_f32 v153, v153\n\t"
    "v_cvt_pk_bf16_f32 v171, v150, v151\n\t"
    "v_mfma_f32_32x32x16_bf16 %2, a[60:63], v[164:167], %2\n\t"
    "v_exp_f32 v154, v154\n\t"
    "v_exp_f32 v155, v155\n\t"
    "v_cvt_pk_bf16_f32 v172, v152, v153\n\t"
    "v_exp_f32 v156, v156\n\t"
    "v_smfmac_f32_16x16x64_bf16 %6, %13, v[160:167], %14\n\t"
    "v_exp_f32 v157, v157\n\t"
    "v_cvt_pk_bf16_f32 v173, v154, v155\n\t"
    "v_exp_f32 v158, v158\n\t"
    "v_exp_f32 v159, v159\n\t"
    "v_cvt_pk_bf16_f32 v174, v156, v157\n\t"
    "v_cvt_pk_bf16_f32 v175, v158, v159\n\t"
    "v_mfma_f32_32x32x16_bf16 v[144:159], a[32:35], a[8:11], 0\n\t"
    "ds_read_b128 a[40:43], %10 offset:28672\n\t"
    "v_exp_f32 v128, v128\n\t"
    "v_exp_f32 v129, v129\n\t"
    "v_exp_f32 v130, v130\n\t"
    "v_exp_f32 v131, v131\n\t"
    "v_mfma_f32_32x32x16_bf16 v[144:159], a[36:39], a[12:15], v[144:159]\n\t"
    "ds_read_b128 a[44:47], %10 offset:29696\n\t"
    "v_cvt_pk_bf16_f32 v160, v128, v129\n\t"
    "v_exp_f32 v132, v132\n\t"
    "v_exp_f32 v133, v133\n\t"
    "v_cvt_pk_bf16_f32 v161, v130, v131\n\t"
    "v_exp_f32 v134, v134\n\t"
    "v_mfma_f32_32x32x16_bf16 %3, a[56:59], v[168:171], %3\n\t"
    "v_exp_f32 v135, v135\n\t"
    "v_cvt_pk_bf16_f32 v162, v132, v133\n\t"
    "v_exp_f32 v136, v136\n\t"
    "v_exp_f32 v137, v137\n\t"
    "v_cvt_pk_bf16_f32 v163, v134, v135\n\t"
    "v_mfma_f32_32x32x16_bf16 %3, a[60:63], v[172:175], %3\n\t"
    "v_exp_f32 v138, v138\n\t"
    "v_exp_f32 v139, v139\n\t"
    "v_cvt_pk_bf16_f32 v164, v136, v137\n\t"
    "v_exp_f32 v140, v140\n\t"
    "v_smfmac_f32_16x16x64_bf16 %7, %13, v[168:175], %14\n\t"
    "v_exp_f32 v141, v141\n\t"
    "v_cvt_pk_bf16_f32 v165, v138, v139\n\t"
    "v_exp_f32 v142, v142\n\t"
    "v_exp_f32 v143, v143\n\t"
    "v_cvt_pk_bf16_f32 v166, v140, v141\n\t"
    "v_cvt_pk_bf16_f32 v167, v142, v143\n\t"
    "v_mfma_f32_32x32x16_bf16 v[128:143], a[32:35], a[16:19], 0\n\t"
    "ds_read_b128 a[56:59], %10 offset:30720\n\t"
    "v_exp_f32 v144, v144\n\t"
    "v_exp_f32 v145, v145\n\t"
    "v_exp_f32 v146, v146\n\t"
    "v_exp_f32 v147, v147\n\t"
    "v_mfma_f32_32x32x16_bf16 v[128:143], a[36:39], a[20:23], v[128:143]\n\t"
    "ds_read_b128 a[60:63], %10 offset:31744\n\t"
    "v_cvt_pk_bf16_f32 v168, v144, v145\n\t"
    "v_exp_f32 v148, v148\n\t"
    "v_exp_f32 v149, v149\n\t"
    "v_cvt_pk_bf16_f32 v169, v146, v147\n\t"
    "v_exp_f32 v150, v150\n\t"
    "v_mfma_f32_32x32x16_bf16 %0, a[48:51], v[160:163], %0\n\t"
    "v_exp_f32 v151, v151\n\t"
    "v_cvt_pk_bf16_f32 v170, v148, v149\n\t"
    "v_exp_f32 v152, v152\n\t"
    "v_exp_f32 v153, v153\n\t"
    "v_cvt_pk_bf16_f32 v171, v150, v151\n\t"
    "v_mfma_f32_32x32x16_bf16 %0, a[52:55], v[164:167], %0\n\t"
    "v_exp_f32 v154, v154\n\t"
    "v_exp_f32 v155, v155\n\t"
    "v_cvt_pk_bf16_f32 v172, v152, v153\n\t"
    "v_exp_f32 v156, v156\n\t"
    "v_smfmac_f32_16x16x64_bf16 %4, %13, v[160:167], %14\n\t"
    "v_exp_f32 v157, v157\n\t"
    "v_cvt_pk_bf16_f32 v173, v154, v155\n\t"
    "v_exp_f32 v158, v158\n\t"
    "v_exp_f32 v159, v159\n\t"
    "v_cvt_pk_bf16_f32 v174, v156, v157\n\t"
    "v_cvt_pk_bf16_f32 v175, v158, v159\n\t"
    "v_mfma_f32_32x32x16_bf16 v[144:159], a[32:35], a[24:27], 0\n\t"
    "v_exp_f32 v128, v128\n\t"
    "v_exp_f32 v129, v129\n\t"
    "v_exp_f32 v130, v130\n\t"
    "v_exp_f32 v131, v131\n\t"
    "v_mfma_f32_32x32x16_bf16 v[144:159], a[36:39], a[28:31], v[144:159]\n\t"
    "v_cvt_pk_bf16_f32 v160, v128, v129\n\t"
    "v_exp_f32 v132, v132\n\t"
    "v_exp_f32 v133, v133\n\t"
    "v_cvt_pk_bf16_f32 v161, v130, v131\n\t"
    "v_exp_f32 v134, v134\n\t"
    "v_mfma_f32_32x32x16_bf16 %1, a[48:51], v[168:171], %1\n\t"
    "v_exp_f32 v135, v135\n\t"
    "v_cvt_pk_bf16_f32 v162, v132, v133\n\t"
    "v_exp_f32 v136, v136\n\t"
    "v_exp_f32 v137, v137\n\t"
    "v_cvt_pk_bf16_f32 v163, v134, v135\n\t"
    "v_mfma_f32_32x32x16_bf16 %1, a[52:55], v[172:175], %1\n\t"
    "v_exp_f32 v138, v138\n\t"
    "v_exp_f32 v139, v139\n\t"
    "v_cvt_pk_bf16_f32 v164, v136, v137\n\t"
    "v_exp_f32 v140, v140\n\t"
    "v_smfmac_f32_16x16x64_bf16 %5, %13, v[168:175], %14\n\t"
    "v_exp_f32 v141, v141\n\t"
    "v_cvt_pk_bf16_f32 v165, v138, v139\n\t"
    "v_exp_f32 v142, v142\n\t"
    "v_exp_f32 v143, v143\n\t"
    "v_cvt_pk_bf16_f32 v166, v140, v141\n\t"
    "v_cvt_pk_bf16_f32 v167, v142, v143\n\t"
    "s_waitcnt lgkmcnt(0)\n\t"
    "v_mfma_f32_32x32x16_bf16 v[128:143], a[40:43], a[0:3], 0\n\t"
    "v_exp_f32 v144, v144\n\t"
    "v_exp_f32 v145, v145\n\t"
    "v_exp_f32 v146, v146\n\t"
    "v_exp_f32 v147, v147\n\t"
    "v_mfma_f32_32x32x16_bf16 v[128:143], a[44:47], a[4:7], v[128:143]\n\t"
    "v_cvt_pk_bf16_f32 v168, v144, v145\n\t"
    "v_exp_f32 v148, v148\n\t"
    "v_exp_f32 v149, v149\n\t"
    "v_cvt_pk_bf16_f32 v169, v146, v147\n\t"
    "v_exp_f32 v150, v150\n\t"
    "v_mfma_f32_32x32x16_bf16 %2, a[48:51], v[160:163], %2\n\t"
    "v_exp_f32 v151, v151\n\t"
    "v_cvt_pk_bf16_f32 v170, v148, v149\n\t"
    "v_exp_f32 v152, v152\n\t"
    "v_exp_f32 v153, v153\n\t"
    "v_cvt_pk_bf16_f32 v171, v150, v151\n\t"
    "v_mfma_f32_32x32x16_bf16 %2, a[52:55], v[164:167], %2\n\t"
    "v_exp_f32 v154, v154\n\t"
    "v_exp_f32 v155, v155\n\t"
    "v_cvt_pk_bf16_f32 v172, v152, v153\n\t"
    "v_exp_f32 v156, v156\n\t"
    "v_smfmac_f32_16x16x64_bf16 %6, %13, v[160:167], %14\n\t"
    "v_exp_f32 v157, v157\n\t"
    "v_cvt_pk_bf16_f32 v173, v154, v155\n\t"
    "v_exp_f32 v158, v158\n\t"
    "v_exp_f32 v159, v159\n\t"
    "v_cvt_pk_bf16_f32 v174, v156, v157\n\t"
    "v_cvt_pk_bf16_f32 v175, v158, v159\n\t"
    "v_mfma_f32_32x32x16_bf16 v[144:159], a[40:43], a[8:11], 0\n\t"
    "s_barrier\n\t"
    "ds_read_b128 a[32:35], %10 offset:0\n\t"
    "v_exp_f32 v128, v128\n\t"
    "v_exp_f32 v129, v129\n\t"
    "v_exp_f32 v130, v130\n\t"
    "v_exp_f32 v131, v131\n\t"
    "v_mfma_f32_32x32x16_bf16 v[144:159], a[44:47], a[12:15], v[144:159]\n\t"
    "ds_read_b128 a[36:39], %10 offset:1024\n\t"
    "v_cvt_pk_bf16_f32 v160, v128, v129\n\t"
    "v_exp_f32 v132, v132\n\t"
    "v_exp_f32 v133, v133\n\t"
    "v_cvt_pk_bf16_f32 v161, v130, v131\n\t"
    "v_exp_f32 v134, v134\n\t"
    "v_mfma_f32_32x32x16_bf16 %3, a[48:51], v[168:171], %3\n\t"
    "v_exp_f32 v135, v135\n\t"
    "v_cvt_pk_bf16_f32 v162, v132, v133\n\t"
    "v_exp_f32 v136, v136\n\t"
    "v_exp_f32 v137, v137\n\t"
    "v_cvt_pk_bf16_f32 v163, v134, v135\n\t"
    "v_mfma_f32_32x32x16_bf16 %3, a[52:55], v[172:175], %3\n\t"
    "v_exp_f32 v138, v138\n\t"
    "v_exp_f32 v139, v139\n\t"
    "v_cvt_pk_bf16_f32 v164, v136, v137\n\t"
    "v_exp_f32 v140, v140\n\t"
    "v_smfmac_f32_16x16x64_bf16 %7, %13, v[168:175], %14\n\t"
    "v_exp_f32 v141, v141\n\t"
    "v_cvt_pk_bf16_f32 v165, v138, v139\n\t"
    "v_exp_f32 v142, v142\n\t"
    "v_exp_f32 v143, v143\n\t"
    "v_cvt_pk_bf16_f32 v166, v140, v141\n\t"
    "v_cvt_pk_bf16_f32 v167, v142, v143\n\t"
    "v_mfma_f32_32x32x16_bf16 v[128:143], a[40:43], a[16:19], 0\n\t"
    "ds_read_b128 a[48:51], %10 offset:2048\n\t"
    "v_exp_f32 v144, v144\n\t"
    "v_exp_f32 v145, v145\n\t"
    "v_exp_f32 v146, v146\n\t"
    "v_exp_f32 v147, v147\n\t"
    "v_mfma_f32_32x32x16_bf16 v[128:143], a[44:47], a[20:23], v[128:143]\n\t"
    "ds_read_b128 a[52:55], %10 offset:3072\n\t"
    "v_cvt_pk_bf16_f32 v168, v144, v145\n\t"
    "v_exp_f32 v148, v148\n\t"
    "v_exp_f32 v149, v149\n\t"
    "v_cvt_pk_bf16_f32 v169, v146, v147\n\t"
    "v_exp_f32 v150, v150\n\t"
    "v_mfma_f32_32x32x16_bf16 %0, a[56:59], v[160:163], %0\n\t"
    "v_exp_f32 v151, v151\n\t"
    "v_cvt_pk_bf16_f32 v170, v148, v149\n\t"
    "v_exp_f32 v152, v152\n\t"
    "v_exp_f32 v153, v153\n\t"
    "v_cvt_pk_bf16_f32 v171, v150, v151\n\t"
    "v_mfma_f32_32x32x16_bf16 %0, a[60:63], v[164:167], %0\n\t"
    "v_exp_f32 v154, v154\n\t"
    "v_exp_f32 v155, v155\n\t"
    "v_cvt_pk_bf16_f32 v172, v152, v153\n\t"
    "v_exp_f32 v156, v156\n\t"
    "v_smfmac_f32_16x16x64_bf16 %4, %13, v[160:167], %14\n\t"
    "v_exp_f32 v157, v157\n\t"
    "v_cvt_pk_bf16_f32 v173, v154, v155\n\t"
    "v_exp_f32 v158, v158\n\t"
    "v_exp_f32 v159, v159\n\t"
    "v_cvt_pk_bf16_f32 v174, v156, v157\n\t"
    "v_cvt_pk_bf16_f32 v175, v158, v159\n\t"
    "v_mfma_f32_32x32x16_bf16 v[144:159], a[40:43], a[24:27], 0\n\t"
    "v_exp_f32 v128, v128\n\t"
    "v_exp_f32 v129, v129\n\t"
    "v_exp_f32 v130, v130\n\t"
    "v_exp_f32 v131, v131\n\t"
    "v_mfma_f32_32x32x16_bf16 v[144:159], a[44:47], a[28:31], v[144:159]\n\t"
    "v_cvt_pk_bf16_f32 v160, v128, v129\n\t"
    "v_exp_f32 v132, v132\n\t"
    "v_exp_f32 v133, v133\n\t"
    "v_cvt_pk_bf16_f32 v161, v130, v131\n\t"
    "v_exp_f32 v134, v134\n\t"
    "v_mfma_f32_32x32x16_bf16 %1, a[56:59], v[168:171], %1\n\t"
    "v_exp_f32 v135, v135\n\t"
    "v_cvt_pk_bf16_f32 v162, v132, v133\n\t"
    "v_exp_f32 v136, v136\n\t"
    "v_exp_f32 v137, v137\n\t"
    "v_cvt_pk_bf16_f32 v163, v134, v135\n\t"
    "v_mfma_f32_32x32x16_bf16 %1, a[60:63], v[172:175], %1\n\t"
    "v_exp_f32 v138, v138\n\t"
    "v_exp_f32 v139, v139\n\t"
    "v_cvt_pk_bf16_f32 v164, v136, v137\n\t"
    "v_exp_f32 v140, v140\n\t"
    "v_smfmac_f32_16x16x64_bf16 %5, %13, v[168:175], %14\n\t"
    "v_exp_f32 v141, v141\n\t"
    "v_cvt_pk_bf16_f32 v165, v138, v139\n\t"
    "v_exp_f32 v142, v142\n\t"
    "v_exp_f32 v143, v143\n\t"
    "v_cvt_pk_bf16_f32 v166, v140, v141\n\t"
    "v_cvt_pk_bf16_f32 v167, v142, v143\n\t"
    "s_waitcnt lgkmcnt(0)\n\t"
    "v_mfma_f32_32x32x16_bf16 v[128:143], a[32:35], a[0:3], 0\n\t"
    "v_exp_f32 v144, v144\n\t"
    "v_exp_f32 v145, v145\n\t"
    "v_exp_f32 v146, v146\n\t"
    "v_exp_f32 v147, v147\n\t"
    "v_mfma_f32_32x32x16_bf16 v[128:143], a[36:39], a[4:7], v[128:143]\n\t"
    "v_cvt_pk_bf16_f32 v168, v144, v145\n\t"
    "v_exp_f32 v148, v148\n\t"
    "v_exp_f32 v149, v149\n\t"
    "v_cvt_pk_bf16_f32 v169, v146, v147\n\t"
    "v_exp_f32 v150, v150\n\t"
    "v_mfma_f32_32x32x16_bf16 %2, a[56:59], v[160:163], %2\n\t"
    "v_exp_f32 v151, v151\n\t"
    "v_cvt_pk_bf16_f32 v170, v148, v149\n\t"
    "v_exp_f32 v152, v152\n\t"
    "v_exp_f32 v153, v153\n\t"
    "v_cvt_pk_bf16_f32 v171, v150, v151\n\t"
    "v_mfma_f32_32x32x16_bf16 %2, a[60:63], v[164:167], %2\n\t"
    "v_exp_f32 v154, v154\n\t"
    "v_exp_f32 v155, v155\n\t"
    "v_cvt_pk_bf16_f32 v172, v152, v153\n\t"
    "v_exp_f32 v156, v156\n\t"
    "v_smfmac_f32_16x16x64_bf16 %6, %13, v[160:167], %14\n\t"
    "v_exp_f32 v157, v157\n\t"
    "v_cvt_pk_bf16_f32 v173, v154, v155\n\t"
    "v_exp_f32 v158, v158\n\t"
    "v_exp_f32 v159, v159\n\t"
    "v_cvt_pk_bf16_f32 v174, v156, v157\n\t"
    "v_cvt_pk_bf16_f32 v175, v158, v159\n\t"
    "s_sub_u32 %8, %8, 1\n\t"
    "s_cmp_lg_u32 %8, 0\n\t"
    "s_cbranch_scc1 LOOP_%=\n\t"
    "s_nop 1\n\t"
    "v_mfma_f32_32x32x16_bf16 %3, a[56:59], v[168:171], %3\n\t"
    "v_smfmac_f32_16x16x64_bf16 %7, %13, v[168:175], %14\n\t"
    "v_mfma_f32_32x32x16_bf16 %3, a[60:63], v[172:175], %3\n\t"
    "s_waitcnt lgkmcnt(0)\n\t"
    "s_nop 7\n\t"
    "s_nop 7\n\t"
    : "+v"(o[0]), "+v"(o[1]), "+v"(o[2]), "+v"(o[3]), "+v"(l[0]), "+v"(l[1]), "+v"(l[2]), "+v"(l[3]), "=&s"(t_cnt), "=&s"(t_ring)
    : "v"(vaddr), "s"(qptr), "v"(vl16), "v"(ones), "v"(idx), "s"(0x07060302u)
    : "memory", "scc", "v128", "v129", "v130", "v131", "v132", "v133", "v134", "v135", "v136", "v137", "v138", "v139", "v140", "v141", "v142", "v143", "v144", "v145", "v146", "v147", "v148", "v149", "v150", "v151", "v152", "v153", "v154", "v155", "v156", "v157", "v158", "v159", "v160", "v161", "v162", "v163", "v164", "v165", "v166", "v167", "v168", "v169", "v170", "v171", "v172", "v173", "v174", "v175", "a0", "a1", "a2", "a3", "a4", "a5", "a6", "a7", "a8", "a9", "a10", "a11", "a12", "a13", "a14", "a15", "a16", "a17", "a18", "a19", "a20", "a21", "a22", "a23", "a24", "a25", "a26", "a27", "a28", "a29", "a30", "a31", "a32", "a33", "a34", "a35", "a36", "a37", "a38", "a39", "a40", "a41", "a42", "a43", "a44", "a45", "a46", "a47", "a48", "a49", "a50", "a51", "a52", "a53", "a54", "a55", "a56", "a57", "a58", "a59", "a60", "a61", "a62", "a63");
}

constexpr int NQ = 4, AT_THREADS = 320, AT_LDS = AT_R * AT_SB, AT_PC = AT_SB / 1024;

__global__ __launch_bounds__(AT_THREADS, 2) void k_attn(char* __restrict__ ws, const char* __restrict__ woa, const char* __restrict__ wob) {
  extern __shared__ __attribute__((aligned(16))) char lds[];
  const int xcd = blockIdx.x & 7, idx = blockIdx.x >> 3, br = idx >> 4, i = idx & 15;
  const int head = xcd * 4 + (i >> 2), quarter = i & 3;
  const int tid = threadIdx.x, lane = tid & 63, wid = rfl(tid >> 6);
  const char* kv = br ? ws + WS_KVB + (size_t)head * 64 * 4096 : ws + WS_KVA;
  const unsigned lds0 = (unsigned)(uintptr_t)lds;
  if (__builtin_expect(wid == 4, 1)) {
    const unsigned lo16 = lane * 16;
    auto issue = [&](int st) {
      const unsigned sl = lds0 + (st % AT_R) * AT_SB; const char* src = kv + st * AT_SB;
#pragma unroll
      for (int p = 0; p < AT_PC; ++p) glds16(src + p * 1024, lo16, sl + p * 1024);
    };
    issue(0); issue(1);
    waitv<AT_PC>();
    asm volatile("s_barrier" ::: "memory");
#pragma unroll
    for (int st = 2; st < AT_R; ++st) issue(st);
#pragma unroll 1
    for (int g = 0; g < AT_NST; ++g) {
      if (g + AT_R - 1 < AT_NST) waitv<(AT_R - 2) * AT_PC>(); else waitv<0>();
      asm volatile("s_barrier" ::: "memory");
      if (g + AT_R < AT_NST) issue(g + AT_R);
      if (g == 2) {
        const char* src = (blockIdx.x < 128 ? woa : wob) + (size_t)(blockIdx.x & 127) * 16384 + lane * 16 + 4096;
        u32x4 d;
        asm volatile("global_load_dwordx4 %0, %1, off offset:-4096\n\tglobal_load_dwordx4 %0, %1, off offset:-3072\n\tglobal_load_dwordx4 %0, %1, off offset:-2048\n\tglobal_load_dwordx4 %0, %1, off offset:-1024\n\t"
                     "global_load_dwordx4 %0, %1, off\n\tglobal_load_dwordx4 %0, %1, off offset:1024\n\tglobal_load_dwordx4 %0, %1, off offset:2048\n\tglobal_load_dwordx4 %0, %1, off offset:3072\n\t"
                     "global_load_dwordx4 %0, %2, off offset:-4096\n\tglobal_load_dwordx4 %0, %2, off offset:-3072\n\tglobal_load_dwordx4 %0, %2, off offset:-2048\n\tglobal_load_dwordx4 %0, %2, off offset:-1024\n\t"
                     "global_load_dwordx4 %0, %2, off\n\tglobal_load_dwordx4 %0, %2, off offset:1024\n\tglobal_load_dwordx4 %0, %2, off offset:2048\n\tglobal_load_dwordx4 %0, %2, off offset:3072\n\t"
                     "s_waitcnt vmcnt(0)" : "=&v"(d) : "v"(src), "v"(src + 8192) : "memory");
      }
    }
    return;
  }
  const int t0 = quarter * 16 + NQ * wid;
  const char* qf = ws + (br ? WS_QB : WS_QA) + ((size_t)head * 64 + t0) * 2048;
  char* o0 = ws + WS_O + (((size_t)br * 64 + t0) * 32 + head) * 2048;
  const int r16 = lane & 15; const bool act = (r16 & 3) < 2; const unsigned one2 = 0x3F803F80u;
  const unsigned a0 = (act && !((r16 >> 2) & 1)) ? one2 : 0u, a1 = (act && ((r16 >> 2) & 1)) ? one2 : 0u;
  const u32x4 ones = {a0, a0, a1, a1};
  const unsigned sidx = (r16 & 1) ? 0xEEEEu : 0x4444u;
  f32x16 o[NQ] = {}; f32x4 l[NQ] = {};
  attn_fast_core(o, l, lds0 + lane * 16, qf + 4096, lane * 16, ones, sidx);
  bool ok = true; float ls[NQ];
#pragma unroll
  for (int b = 0; b < NQ; ++b) {
    ls[b] = l[b][0] + l[b][1];
    float z0 = 0.f, z1 = 0.f, z2 = 0.f, z3 = 0.f;
#pragma unroll
    for (int r = 0; r < 16; r += 4) { z0 = __builtin_fmaf(o[b][r], 0.f, z0); z1 = __builtin_fmaf(o[b][r + 1], 0.f, z1); z2 = __builtin_fmaf(o[b][r + 2], 0.f, z2); z3 = __builtin_fmaf(o[b][r + 3], 0.f, z3); }
    ok = ok && (ls[b] >= 0x1p-30f) && (ls[b] <= 0x1p60f) && ((z0 + z1) + (z2 + z3) == 0.f);
  }
  if (__builtin_expect(__all(ok), 1)) {
#pragma unroll
    for (int b = 0; b < NQ; ++b) {
      const float r = __builtin_amdgcn_rcpf(ls[b]); char* op = o0 + b * 32 * 2048 + lane * 16;
      st16(op, pack8(o[b], 0, r)); st16(op + 1024, pack8(o[b], 1, r));
    }
  } else {
#pragma unroll 1
    for (int b = 0; b < NQ; ++b) attn_exact(qf + b * 2048, kv, o0 + b * 32 * 2048, lane);
  }
}

constexpr int O_XB = 16384, O_WB = 16384, O_SLOT = O_XB + O_WB, O_LDS = 4 * O_SLOT;
__global__ __launch_bounds__(512, 2) void k_out(const char* __restrict__ ws, const char* __restrict__ woa, const char* __restrict__ wob, char* __restrict__ out) {
  extern __shared__ __attribute__((aligned(16))) char lds[];
  const int xcd = blockIdx.x & 7, idx = blockIdx.x >> 3, br = xcd >> 2, mb = 4 * (xcd & 3) + (idx >> 3), nb = idx & 7;
  const int tid = threadIdx.x, lane = tid & 63, wid = rfl(tid >> 6);
  const unsigned lds0 = (unsigned)(uintptr_t)lds;
  if (__builtin_expect(wid >= 4, 1)) {
    const int lw = wid - 4;
    const char* xbase = ws + WS_O + ((size_t)br * 64 + mb * 4 + lw) * 65536;
    const char* W = (br ? wob : woa) + nb * 256;
    unsigned wo[4];
#pragma unroll
    for (int i = 0; i < 4; ++i) wo[i] = (16 * lw + 4 * i + (lane >> 4)) * 2048u + (((lane & 15) ^ (((lane >> 4) << 2) | ((i & 1) << 1) | (i >> 1))) << 4);
    const unsigned lo16 = lane * 16;
    auto issue = [&](int st) {
      const unsigned slot = lds0 + (st & 3) * O_SLOT;
#pragma unroll
      for (int i = 0; i < 4; ++i) glds16(xbase + st * 4096 + i * 1024, lo16, slot + lw * 4096 + i * 1024);
      const char* wb = W + (size_t)st * 64 * 2048;
#pragma unroll
      for (int i = 0; i < 4; ++i) glds16(wb, wo[i], slot + O_XB + lw * 4096 + i * 1024);
    };
    issue(0); issue(1);
    waitv<8>(); bar_l();
    issue(2); issue(3);
#pragma unroll 1
    for (int j = 0; j < 16; ++j) {
      if (j >= 1 && j + 3 < 16) issue(j + 3);
      if (j + 3 < 16) waitv<16>(); else if (j + 2 < 16) waitv<8>(); else waitv<0>();
      bar_l();
    }
    return;
  }
  const int wm = wid >> 1, wn = wid & 1, t16 = lane & 15, g = lane >> 4, q = t16 >> 2, p = lane & 3;
  unsigned wa[8], xa[1];
#pragma unroll
  for (int c = 0; c < 4; ++c)
#pragma unroll
    for (int t = 0; t < 2; ++t)
      wa[2 * c + t] = lds0 + (16 * (g >> 1) + 8 * t + 4 * (g & 1) + q) * 256 + (((8 * wn + 2 * c + (p >> 1)) ^ ((q << 2) | ((g & 1) << 1) | t)) << 4) + 8 * (p & 1);
  xa[0] = lds0 + (2 * wm) * 4096 + (g >> 1) * 1024 + (g & 1) * 512 + t16 * 16;
  f32x16 acc[4] = {};
  out_core(acc, xa, wa);
  char* stg = lds + wid * 8192;
#pragma unroll
  for (int i = 0; i < 4; ++i)
#pragma unroll
    for (int c = 0; c < 4; ++c) {
      const int row = 16 * i + t16, ch = (2 * c + (g >> 1)) ^ (row & 7);
      u32x2 v = {cvtpk(acc[i][4 * c], acc[i][4 * c + 1]), cvtpk(acc[i][4 * c + 2], acc[i][4 * c + 3])};
      *(u32x2*)(stg + row * 128 + ch * 16 + (g & 1) * 8) = v;
    }
  char* o = out + (size_t)(mb * 128 + wm * 64 + (lane >> 3)) * 4096 + br * 2048 + nb * 256 + wn * 128 + (lane & 7) * 16;
#pragma unroll
  for (int j = 0; j < 8; ++j) {
    const int row = 8 * j + (lane >> 3);
    st16(o + (size_t)j * 8 * 4096, *(const u32x4*)(stg + row * 128 + (((lane & 7) ^ (row & 7)) << 4)));
  }
}

extern "C" void kernel_launch(void* const* d_in, const int* in_sizes, int n_in, void* d_out, int out_size, void* d_ws, size_t ws_size, hipStream_t stream) {
  (void)in_sizes; (void)n_in; (void)out_size; (void)ws_size;
  (void)hipFuncSetAttribute((const void*)k_proj, hipFuncAttributeMaxDynamicSharedMemorySize, P_LDS);
  (void)hipFuncSetAttribute((const void*)k_attn, hipFuncAttributeMaxDynamicSharedMemorySize, AT_LDS);
  (void)hipFuncSetAttribute((const void*)k_out, hipFuncAttributeMaxDynamicSharedMemorySize, O_LDS);
  char* ws = (char*)d_ws;
  k_proj<<<256, 512, P_LDS, stream>>>((const char*)d_in[0], (const char*)d_in[1], (const char*)d_in[2], (const char*)d_in[3], (const char*)d_in[5], (const char*)d_in[6], ws);
  k_attn<<<256, AT_THREADS, AT_LDS, stream>>>(ws, (const char*)d_in[4], (const char*)d_in[7]);
  k_out<<<256, 512, O_LDS, stream>>>(ws, (const char*)d_in[4], (const char*)d_in[7], (char*)d_out);
}
```
